```python
import torch

def _softmax(x, axis=-1):
    return torch.softmax(x, dim=axis)

def _gelu(x):
    return torch.nn.functional.gelu(x, approximate='tanh')

def _silu(x):
    return torch.nn.functional.silu(x)

def _sigmoid(x):
    return torch.sigmoid(x)

def _layernorm(x, axis=-1, eps=1e-05):
    m = x.mean(dim=axis, keepdim=True)
    v = x.var(dim=axis, keepdim=True, unbiased=False)
    return (x - m) / torch.sqrt(v + eps)

def _scatter_add(tgt, idx, src, axis=0):
    return tgt.clone().index_add_(axis, idx, src)

def _segment_sum(src, seg, n):
    out = torch.zeros((n,) + src.shape[1:], dtype=src.dtype)
    return out.index_add_(0, seg, src)

def _pad(x, pw):
    flat = tuple((v for pair in reversed(pw) for v in pair))
    return torch.nn.functional.pad(x, flat)
kernel_name = 'parallel_sum__memory_aug_recurrent_mem_tr__dense_activation_relu__prompt'

def setup_inputs():

    _w = lambda a: (lambda t: t.to(torch.bfloat16) if t.is_floating_point() else t)(torch.as_tensor(a))

    def _A_setup():
        B, S, Dm = (1, 2048, 448)
        H, Dh = (16, 256)
        M, seg = (8, 32)
        seg = min(seg, S)
        S = S // seg * seg
        return {'x': _w((lambda *s: torch.randn(*s, dtype=torch.bfloat16) * 0.05)(B, S, Dm)), 'mem0': _w((lambda *s: torch.randn(*s, dtype=torch.bfloat16) * 1.6)(B, M, Dm)), 'Wq': _w((lambda *s: torch.randn(*s, dtype=torch.bfloat16) * 0.8)(Dm, H, Dh) * Dm ** (-0.5)), 'Wk': _w((lambda *s: torch.randn(*s, dtype=torch.bfloat16) * 0.8)(Dm, H, Dh) * Dm ** (-0.5)), 'Wv': _w((lambda *s: torch.randn(*s, dtype=torch.bfloat16) * 1.6)(Dm, H, Dh) * Dm ** (-0.5)), 'Wo': _w((lambda *s: torch.randn(*s, dtype=torch.bfloat16) * 1.6)(H, Dh, Dm) * (H * Dh) ** (-0.5)), 'seg_len': seg}
    _A_in = _A_setup()

    def _B_setup():
        B, S, D, Dff = (1, 2048, 448, 1792)
        return {'x': _w((lambda *s: torch.randn(*s, dtype=torch.bfloat16) * 0.05)(B, S, D)), 'w1': _w((lambda *s: torch.randn(*s, dtype=torch.bfloat16) * 0.05)(D, Dff)), 'w2': _w((lambda *s: torch.randn(*s, dtype=torch.bfloat16) * 0.05)(Dff, D))}
    _B_in = _B_setup()
    return {'A_x': _A_in['x'], 'A_mem0': _A_in['mem0'], 'A_Wq': _A_in['Wq'], 'A_Wk': _A_in['Wk'], 'A_Wv': _A_in['Wv'], 'A_Wo': _A_in['Wo'], 'A_seg_len': _A_in['seg_len'], 'B_x': _B_in['x'], 'B_w1': _B_in['w1'], 'B_w2': _B_in['w2']}

def reference(A_x, A_mem0, A_Wq, A_Wk, A_Wv, A_Wo, A_seg_len, B_x, B_w1, B_w2):

    def _A_ref(x, mem0, Wq, Wk, Wv, Wo, seg_len):
        B, S, Dm = x.shape
        H, Dh = (Wq.shape[1], Wq.shape[2])
        M = mem0.shape[1]
        scale = Dh ** (-0.5)
        n_seg = S // seg_len
        L = M + seg_len
        idx = torch.arange(L)
        mask = (idx[:, None] < M) | (idx[:, None] >= idx[None, :])
        mem = mem0
        outs = []
        for s in range(n_seg):
            xs = x[:, s * seg_len:(s + 1) * seg_len, :].clone()
            h = torch.concatenate([mem, xs], axis=1)
            q = torch.einsum('btd,dhe->bhte', h, Wq)
            k = torch.einsum('btd,dhe->bhte', h, Wk)
            v = torch.einsum('btd,dhe->bhte', h, Wv)
            scores = torch.einsum('bhqe,bhke->bhqk', q, k) * scale
            scores = torch.where(mask[None, None], scores, -1000000000.0)
            p = _softmax(scores, axis=-1)
            o = torch.einsum('bhqk,bhke->bhqe', p, v)
            o = torch.einsum('bhte,hed->btd', o, Wo)
            mem = o[:, :M, :].clone()
            outs.append(o[:, M:, :])
        return torch.concatenate(outs, axis=1)

    def _B_ref(x, w1, w2):
        h = torch.einsum('bsd,df->bsf', x, w1)
        zero = torch.zeros_like(h)
        h = torch.maximum(h, zero)
        return torch.einsum('bsf,fd->bsd', h, w2)
    x = A_x
    xn = _layernorm(x, axis=-1)
    return x + _A_ref(xn, A_mem0, A_Wq, A_Wk, A_Wv, A_Wo, A_seg_len) + _B_ref(xn, B_w1, B_w2)
```

```cpp
#include <hip/hip_runtime.h>
#include <stdint.h>

typedef __bf16 bf16x2 __attribute__((ext_vector_type(2)));
typedef __bf16 bf16x8 __attribute__((ext_vector_type(8)));
typedef __bf16 bf16x16 __attribute__((ext_vector_type(16)));
typedef float f32x2 __attribute__((ext_vector_type(2)));
typedef float f32x4 __attribute__((ext_vector_type(4)));
typedef float f32x16 __attribute__((ext_vector_type(16)));
typedef unsigned v2u __attribute__((ext_vector_type(2)));
typedef unsigned v4u __attribute__((ext_vector_type(4)));
typedef unsigned v8u __attribute__((ext_vector_type(8)));
typedef unsigned char uchar;
typedef __amdgpu_buffer_rsrc_t rsrc_t;
#define DEV __device__ __forceinline__
#define LDSP __attribute__((address_space(3)))

constexpr int NSEG = 64;
constexpr unsigned SENT = 0xFFFFFFFFu;
constexpr unsigned SPIN_LIMIT = 1u << 18;

constexpr unsigned OFF_EX = 0;
constexpr unsigned OFF_XA = OFF_EX + (6u << 20);
constexpr unsigned OFF_XC = OFF_XA + 1835008u;
constexpr unsigned OFF_XF = OFF_XC + 1835008u;
constexpr unsigned OFF_WFA = OFF_XF + 2097152u;
constexpr unsigned OFF_WFB = OFF_WFA + 6422528u;
constexpr unsigned OFF_W1F = OFF_WFB + 6422528u;
constexpr unsigned OFF_W2F = OFF_W1F + 1605632u;
constexpr unsigned OFF_WOT = OFF_W2F + 1605632u;
constexpr unsigned OFF_T = OFF_WOT + 3670016u;
constexpr unsigned OFF_YX = OFF_T + 29360128u;
constexpr unsigned OFF_HID = OFF_YX + 29360128u;
constexpr unsigned OFF_ZH = OFF_HID + 7340032u;
constexpr unsigned OFF_PX = OFF_ZH + 7340032u;
constexpr unsigned OFF_CTL = OFF_PX + 2621440u;
constexpr unsigned WS_USED = OFF_CTL + 4096u;

DEV rsrc_t mk_rsrc(const void* p, unsigned bytes) { return __builtin_amdgcn_make_buffer_rsrc((void*)p, 0, bytes, 0x00020000); }
DEV v4u ld16(rsrc_t r, unsigned off) { return __builtin_amdgcn_raw_buffer_load_b128(r, off, 0, 0); }
DEV v4u ld16_sc1(rsrc_t r, unsigned off) { return __builtin_amdgcn_raw_buffer_load_b128(r, off, 0, 16); }
DEV void st16(rsrc_t r, unsigned off, v4u v) { __builtin_amdgcn_raw_buffer_store_b128(v, r, off, 0, 0); }
DEV void st8(rsrc_t r, unsigned off, v2u v) { __builtin_amdgcn_raw_buffer_store_b64(v, r, off, 0, 0); }
DEV void st4_sc1(rsrc_t r, unsigned off, unsigned v) { __builtin_amdgcn_raw_buffer_store_b32(v, r, off, 0, 16); }
DEV unsigned pk(float a, float b) { bf16x2 v = {(__bf16)a, (__bf16)b}; return __builtin_bit_cast(unsigned, v); }
DEV float asf(unsigned u) { return __builtin_bit_cast(float, u); }
DEV float bflo(unsigned u) { return __builtin_bit_cast(float, u << 16); }
DEV float bfhi(unsigned u) { return __builtin_bit_cast(float, u & 0xFFFF0000u); }
DEV int swap23(int r) { return (r & 0x13) | ((r & 4) << 1) | ((r & 8) >> 1); }
DEV int kap(int p) { return (p & ~15) | (((p >> 1) & 3) << 2) | (((p >> 3) & 1) << 1) | (p & 1); }
DEV int kapinv16(int n) { return (((n >> 1) & 1) << 3) | ((n >> 2) << 1) | (n & 1); }
DEV bf16x8 asb8(v4u v) { return __builtin_bit_cast(bf16x8, v); }
DEV bf16x16 cat16(v4u a, v4u b) { v8u r = {a.x, a.y, a.z, a.w, b.x, b.y, b.z, b.w}; return __builtin_bit_cast(bf16x16, r); }
DEV void wait_vm0() { asm volatile("s_waitcnt vmcnt(0)" ::: "memory"); }
DEV void wait_lgkm0() { asm volatile("s_waitcnt lgkmcnt(0)" ::: "memory"); }
DEV void raw_barrier() { asm volatile("s_waitcnt lgkmcnt(0)\n\ts_barrier" ::: "memory"); }
DEV void dma16(rsrc_t r, unsigned voff, unsigned ldsbase) {
    asm volatile("s_mov_b32 m0, %0\n\ts_nop 0\n\tbuffer_load_dwordx4 %1, %2, 0 offen lds" ::"s"(ldsbase), "v"(voff), "s"(r) : "m0", "memory");
}
DEV unsigned rfl(unsigned v) { return __builtin_amdgcn_readfirstlane(v); }
DEV v4u lds_ld16(const uchar* lds, unsigned off) { return *(const v4u*)(lds + off); }
DEV unsigned ex_win(unsigned posbits, int wd) { return OFF_EX + (unsigned)wd * 32768u + ((posbits >> (2 * (wd >> 4))) & 3u) * 8192u; }

__global__ void __launch_bounds__(256) k_prep(const __bf16* __restrict__ x, const __bf16* __restrict__ w1, const __bf16* __restrict__ w2,
                                              const __bf16* __restrict__ wo, uchar* __restrict__ ws) {
    const int bid = blockIdx.x, tid = threadIdx.x, lane = tid & 63, wave = tid >> 6;
    if (bid < 512) {
        const int row = bid * 4 + wave, s = row >> 5, r5 = row & 31;
        v4u v = {0u, 0u, 0u, 0u};
        if (lane < 56) v = *(const v4u*)(x + row * 448 + lane * 8);
        float f[8] = {bflo(v.x), bfhi(v.x), bflo(v.y), bfhi(v.y), bflo(v.z), bfhi(v.z), bflo(v.w), bfhi(v.w)};
        float sum = 0.f;
        for (int i = 0; i < 8; ++i) sum += f[i];
        for (int o = 32; o; o >>= 1) sum += __shfl_xor(sum, o);
        const float mean = sum * (1.0f / 448.0f);
        float sq = 0.f;
        for (int i = 0; i < 8; ++i) { f[i] -= mean; sq += f[i] * f[i]; }
        if (lane >= 56) sq = 0.f;
        for (int o = 32; o; o >>= 1) sq += __shfl_xor(sq, o);
        const float rstd = 1.0f / sqrtf(sq * (1.0f / 448.0f) + 1e-5f);
        const unsigned d0 = pk(f[0] * rstd, f[1] * rstd), d1 = pk(f[2] * rstd, f[3] * rstd), d2 = pk(f[4] * rstd, f[5] * rstd), d3 = pk(f[6] * rstd, f[7] * rstd);
        const bool odd = lane & 1;
        const unsigned s0 = odd ? d0 : d1, s1 = odd ? d2 : d3;
        const unsigned r0 = __shfl_xor(s0, 1), r1 = __shfl_xor(s1, 1);
        const v4u p = odd ? (v4u){r0, r1, d1, d3} : (v4u){d0, d2, r0, r1};
        if (lane < 56) {
            *(v4u*)(ws + OFF_XA + s * 28672 + (lane >> 1) * 1024 + ((lane & 1) * 32 + r5) * 16) = (v4u){d0, d1, d2, d3};
            *(v4u*)(ws + OFF_XC + s * 28672 + lane * 512 + r5 * 16) = p;
            const int w = lane / 14, cl = lane % 14, tau = r5 >> 4, col = kapinv16(r5 & 15);
            uchar* base = ws + OFF_XF + (s * 4 + w) * 8192;
            if (cl < 8) *(v4u*)(base + ((0 * 2 + tau) * 2 + (cl >> 2)) * 1024 + ((cl & 3) * 16 + col) * 16) = p;
            else if (cl < 12) { const int g = cl - 8; *(v4u*)(base + ((2 + tau) * 2 + (g >> 1)) * 1024 + ((2 * (g & 1)) * 16 + col) * 16) = p; }
            else {
                uchar* b2 = base + ((2 + tau) * 2 + (cl - 12)) * 1024 + col * 16;
                *(v4u*)(b2 + 1 * 256) = (v4u){p.x, p.y, 0u, 0u};
                *(v4u*)(b2 + 3 * 256) = (v4u){p.z, p.w, 0u, 0u};
            }
        }
    } else if (bid < 512 + 392) {
        const int p = (bid - 512) * 256 + tid, fb = p / (28 * 64), kk = (p >> 6) % 28, l = p & 63;
        const int f = 32 * fb + swap23(l & 31), d0 = 16 * kk + 8 * (l >> 5);
        const unsigned short* src = (const unsigned short*)w1 + d0 * 1792 + f;
        unsigned e[8];
        for (int j = 0; j < 8; ++j) e[j] = src[j * 1792];
        *(v4u*)(ws + OFF_W1F + p * 16) = (v4u){e[0] | (e[1] << 16), e[2] | (e[3] << 16), e[4] | (e[5] << 16), e[6] | (e[7] << 16)};
    } else if (bid < 512 + 784) {
        const int p = (bid - 904) * 256 + tid, b = p / (112 * 64), kk = (p >> 6) % 112, l = p & 63;
        const int n = 32 * b + swap23(l & 31), f0 = 16 * kk + 8 * (l >> 5);
        const unsigned short* src = (const unsigned short*)w2 + f0 * 448 + n;
        unsigned e[8];
        for (int j = 0; j < 8; ++j) e[j] = src[j * 448];
        *(v4u*)(ws + OFF_W2F + p * 16) = (v4u){e[0] | (e[1] << 16), e[2] | (e[3] << 16), e[4] | (e[5] << 16), e[6] | (e[7] << 16)};
    } else {
        const int p = (bid - 1296) * 256 + tid, h = p / (32 * 448), ec = (p / 448) % 32, c = p % 448;
        const unsigned short* src = (const unsigned short*)wo + (h * 256 + 8 * ec) * 448 + c;
        unsigned e[8];
        for (int j = 0; j < 8; ++j) e[j] = src[j * 448];
        *(v4u*)(ws + OFF_WOT + ((h * 448 + c) * 256 + 8 * ec) * 2) = (v4u){e[0] | (e[1] << 16), e[2] | (e[3] << 16), e[4] | (e[5] << 16), e[6] | (e[7] << 16)};
    }
}

DEV int philoc(int tt, int nl) {
    if (tt < 4) return 16 * (nl >> 2) + 8 * (tt >> 1) + 4 * ((nl >> 1) & 1) + 2 * (tt & 1) + (nl & 1);
    if (tt < 6) return 64 + 8 * (nl >> 2) + 4 * ((nl >> 1) & 1) + 2 * (tt - 4) + (nl & 1);
    return 96 + nl;
}
__global__ void __launch_bounds__(256) k_wprod(const __bf16* __restrict__ wq, const __bf16* __restrict__ wk, const __bf16* __restrict__ wv,
                                               const __bf16* __restrict__ mem0, uchar* __restrict__ ws) {
    const int bid = blockIdx.x, tid = threadIdx.x, lane = tid & 63, wave = tid >> 6;
    if (bid < 224) {
        const int type = bid / 112, h = (bid % 112) / 7, ds = bid % 7;
        const int r = lane & 31, hh = lane >> 5;
        const __bf16* wa = type ? wv : wq;
        bf16x8 A[2][16];
#pragma unroll
        for (int dt = 0; dt < 2; ++dt) {
            const __bf16* p = wa + (64 * ds + 32 * dt + swap23(r)) * 4096 + h * 256 + 8 * hh;
#pragma unroll
            for (int kk = 0; kk < 16; ++kk) A[dt][kk] = *(const bf16x8*)(p + 16 * kk);
        }
        const float scale = type ? 1.0f : 0.09016844005556021f;
        for (int ft = wave; ft < 14; ft += 4) {
            const int rho = 32 * ft + r;
            const __bf16* pb;
            unsigned dst;
            if (type == 0) {
                const int q = rho >> 4, w = q / 7, tt = q % 7, nl = rho & 15;
                const int f = kap(112 * w + philoc(tt, nl));
                pb = wk + f * 4096 + h * 256 + 8 * hh;
                dst = OFF_WFA + ((((h * 4 + w) * 7 + tt) * 7 + ds) * 2) * 1024 + nl * 16;
            } else {
                const int c = kap(rho), ct = r >> 4, nl = r & 15, hl = h & 3;
                pb = (const __bf16*)(ws + OFF_WOT) + (h * 448 + c) * 256 + 8 * hh;
                const int tl = (2 * (hl >> 1) + ct) * 2 + (hl & 1);
                dst = OFF_WFB + ((((ft * 4 + (h >> 2)) * 8 + tl) * 7 + ds) * 2) * 1024 + nl * 16;
            }
            bf16x8 B[16];
#pragma unroll
            for (int kk = 0; kk < 16; ++kk) B[kk] = *(const bf16x8*)(pb + 16 * kk);
            f32x16 acc[2];
#pragma unroll
            for (int dt = 0; dt < 2; ++dt) {
#pragma unroll
                for (int i = 0; i < 16; ++i) acc[dt][i] = 0.f;
#pragma unroll
                for (int kk = 0; kk < 16; ++kk) acc[dt] = __builtin_amdgcn_mfma_f32_32x32x16_bf16(A[dt][kk], B[kk], acc[dt], 0, 0, 0);
            }
#pragma unroll
            for (int dt = 0; dt < 2; ++dt)
#pragma unroll
                for (int x = 0; x < 2; ++x) {
                    const f32x16& a = acc[dt];
                    v4u o = {pk(a[8 * x] * scale, a[8 * x + 1] * scale), pk(a[8 * x + 2] * scale, a[8 * x + 3] * scale),
                             pk(a[8 * x + 4] * scale, a[8 * x + 5] * scale), pk(a[8 * x + 6] * scale, a[8 * x + 7] * scale)};
                    *(v4u*)(ws + dst + dt * 1024 + (2 * x + hh) * 256) = o;
                }
        }
    } else {
        const int wd = bid - 224;
        const unsigned posbits = 0;
        uchar* base = ws + ex_win(posbits, wd);
        for (int q = tid; q < 512; q += 256) {
            v4u v = {SENT, SENT, SENT, SENT};
            if (wd == 0 && q < 448) {
                const int xch = q >> 3, i = q & 7;
                const unsigned short* m = (const unsigned short*)mem0 + i * 448;
                unsigned e[8];
                for (int j = 0; j < 8; ++j) e[j] = m[kap(8 * xch + j)];
                v = (v4u){e[0] | (e[1] << 16), e[2] | (e[3] << 16), e[4] | (e[5] << 16), e[6] | (e[7] << 16)};
                v.x = min(v.x, 0xFFFFFFFEu); v.y = min(v.y, 0xFFFFFFFEu); v.z = min(v.z, 0xFFFFFFFEu); v.w = min(v.w, 0xFFFFFFFEu);
            }
            *(v4u*)(base + q * 16) = v;
        }
    }
}

DEV unsigned wfa_piece(int h, int phi, int dc) {
    const int w = phi / 112, pl = phi % 112;
    int tt, nl;
    if (pl < 64) { nl = 4 * (pl >> 4) + 2 * ((pl >> 2) & 1) + (pl & 1); tt = 2 * ((pl >> 3) & 1) + ((pl >> 1) & 1); }
    else if (pl < 96) { const int u = pl - 64; nl = 4 * (u >> 3) + 2 * ((u >> 2) & 1) + (u & 1); tt = 4 + ((u >> 1) & 1); }
    else { tt = 6; nl = pl - 96; }
    return OFF_WFA + ((((h * 4 + w) * 7 + tt) * 7 + (dc >> 3)) * 2 + ((dc >> 2) & 1)) * 1024 + ((dc & 3) * 16 + nl) * 16;
}
template <int TYPE>
DEV void bulk_task(rsrc_t R, uchar* lds, int h, int cb) {
    const int tid = threadIdx.x, lane = tid & 63, wave = rfl(tid >> 6);
    const int sg = wave >> 1, ch = wave & 1, r = lane & 31, hh = lane >> 5;
    const unsigned ldsb = (unsigned)(uintptr_t)(LDSP uchar*)lds;
    bf16x8 W[28];
#pragma unroll
    for (int kk = 0; kk < 28; ++kk) {
        unsigned off;
        const int dc = 2 * kk + hh;
        if (TYPE == 0) off = wfa_piece(h, 64 * cb + 32 * ch + swap23(r), dc);
        else if (TYPE == 1) {
            const int b = 2 * cb + ch, ct = r >> 4, nl = r & 15, hl = h & 3, tl = (2 * (hl >> 1) + ct) * 2 + (hl & 1);
            off = OFF_WFB + ((((b * 4 + (h >> 2)) * 8 + tl) * 7 + (dc >> 3)) * 2 + ((dc >> 2) & 1)) * 1024 + ((dc & 3) * 16 + nl) * 16;
        } else off = OFF_W1F + ((2 * cb + ch) * 28 + kk) * 1024 + lane * 16;
        W[kk] = asb8(ld16(R, off));
    }
    auto stage = [&](int it) {
        const unsigned src = OFF_XA + it * 57344 + wave * 14336 + lane * 16;
        const unsigned dst = ldsb + (it & 1) * 57344 + wave * 14336;
#pragma unroll
        for (int q = 0; q < 14; ++q) dma16(R, src + q * 1024, dst + q * 1024);
    };
    stage(0);
    const unsigned rdoff = sg * 28672 + ((TYPE == 1) ? (hh * 32 + swap23(r)) * 16 : lane * 16);
    for (int it = 0; it < 32; ++it) {
        wait_vm0();
        raw_barrier();
        if (it + 1 < 32) stage(it + 1);
        const uchar* xb = lds + (it & 1) * 57344 + rdoff;
        f32x16 acc;
#pragma unroll
        for (int i = 0; i < 16; ++i) acc[i] = 0.f;
#pragma unroll
        for (int kk = 0; kk < 28; ++kk) {
            const bf16x8 xf = *(const bf16x8*)(xb + kk * 1024);
            if (TYPE == 1) acc = __builtin_amdgcn_mfma_f32_32x32x16_bf16(xf, W[kk], acc, 0, 0, 0);
            else acc = __builtin_amdgcn_mfma_f32_32x32x16_bf16(W[kk], xf, acc, 0, 0, 0);
        }
        const int s = 2 * it + sg;
#pragma unroll
        for (int x = 0; x < 2; ++x) {
            float a[8];
#pragma unroll
            for (int i = 0; i < 8; ++i) a[i] = (TYPE == 2) ? fmaxf(acc[8 * x + i], 0.f) : acc[8 * x + i];
            const v4u o = {pk(a[0], a[1]), pk(a[2], a[3]), pk(a[4], a[5]), pk(a[6], a[7])};
            unsigned off;
            if (TYPE == 0) off = OFF_T + ((s * 16 + h) * 56 + 8 * cb + 4 * ch + 2 * x + hh) * 512 + r * 16;
            else if (TYPE == 1) off = OFF_YX + ((((s * 14 + 2 * cb + ch) * 2 + (r >> 4)) * 16 + h) * 4 + 2 * x + hh) * 256 + (r & 15) * 16;
            else off = OFF_HID + (s * 224 + 8 * cb + 4 * ch + 2 * x + hh) * 512 + r * 16;
            st16(R, off, o);
        }
    }
}
__global__ void __launch_bounds__(256, 1) k_bulk(uchar* __restrict__ ws) {
    extern __shared__ __attribute__((aligned(16))) uchar lds[];
    const rsrc_t R = mk_rsrc(ws, WS_USED);
    const int bid = blockIdx.x;
    if (bid < 112) bulk_task<1>(R, lds, bid / 7, bid % 7);
    else if (bid < 224) bulk_task<0>(R, lds, (bid - 112) / 7, (bid - 112) % 7);
    else bulk_task<2>(R, lds, 0, bid - 224);
}

DEV unsigned umax3(unsigned a, unsigned b, unsigned c) { return max(max(a, b), c); }
template <int N, int SPLIT>
DEV bool poll_frags(rsrc_t R, unsigned lane16, unsigned s0, unsigned s1, v4u (&m)[N]) {
    unsigned spins = 0;
    for (;;) {
        asm volatile("" ::: "memory");
#pragma unroll
        for (int k = 0; k < N; ++k) {
            const int kk = (k < SPLIT) ? k : k - SPLIT;
            m[k] = __builtin_amdgcn_raw_buffer_load_b128(R, lane16 + (kk & 3) * 1024, ((k < SPLIT) ? s0 : s1) + (kk >> 2) * 4096, 16);
        }
        unsigned mx = 0;
#pragma unroll
        for (int k = 0; k < N; ++k) mx = umax3(umax3(mx, m[k].x, m[k].y), m[k].z, m[k].w);
        if (__all(mx != SENT)) return true;
        if (++spins > SPIN_LIMIT) return false;
    }
}
DEV float dpp_ror(float v, int n) {
    const int iv = __builtin_bit_cast(int, v);
    int r;
    switch (n) {
        case 8: r = __builtin_amdgcn_update_dpp(iv, iv, 0x128, 0xf, 0xf, false); break;
        case 4: r = __builtin_amdgcn_update_dpp(iv, iv, 0x124, 0xf, 0xf, false); break;
        case 2: r = __builtin_amdgcn_update_dpp(iv, iv, 0x122, 0xf, 0xf, false); break;
        default: r = __builtin_amdgcn_update_dpp(iv, iv, 0x121, 0xf, 0xf, false); break;
    }
    return __builtin_bit_cast(float, r);
}
DEV float fold32(float a, float b) {
    const auto p = __builtin_amdgcn_permlane32_swap(__builtin_bit_cast(unsigned, a), __builtin_bit_cast(unsigned, b), false, false);
    const unsigned p0 = p[0], p1 = p[1];
    return asf(p0) + asf(p1);
}
DEV float half_max(float v) {
    v = fmaxf(v, dpp_ror(v, 8)); v = fmaxf(v, dpp_ror(v, 4)); v = fmaxf(v, dpp_ror(v, 2)); v = fmaxf(v, dpp_ror(v, 1));
    const unsigned u = __builtin_bit_cast(unsigned, v);
    const auto p = __builtin_amdgcn_permlane16_swap(u, u, false, false);
    const unsigned p0 = p[0], p1 = p[1];
    return fmaxf(asf(p0), asf(p1));
}
DEV float half_sum(float v) {
    v += dpp_ror(v, 8); v += dpp_ror(v, 4); v += dpp_ror(v, 2); v += dpp_ror(v, 1);
    const unsigned u = __builtin_bit_cast(unsigned, v);
    const auto p = __builtin_amdgcn_permlane16_swap(u, u, false, false);
    const unsigned p0 = p[0], p1 = p[1];
    return asf(p0) + asf(p1);
}
#define SMF(a, b, c, idx) __builtin_amdgcn_smfmac_f32_16x16x64_bf16(a, b, c, idx, 0, 0)

constexpr unsigned LA_W = 0;
constexpr unsigned LA_X = 57344;
constexpr unsigned LA_M = LA_X + 65536;
constexpr unsigned LA_S = LA_M + 16384;
constexpr unsigned LA_END = LA_S + 13312;
DEV void chain_A(rsrc_t R, uchar* lds, int h, unsigned posbits) {
    const int tid = threadIdx.x, lane = tid & 63, w = rfl(tid >> 6);
    const unsigned ldsb = (unsigned)(uintptr_t)(LDSP uchar*)lds;
    bf16x16 W[7][6];
    const unsigned wbase = OFF_WFA + (h * 4 + w) * (7 * 7 * 2048) + lane * 16;
#pragma unroll
    for (int tt = 0; tt < 7; ++tt) {
#pragma unroll
        for (int ks = 0; ks < 6; ++ks) W[tt][ks] = cat16(ld16(R, wbase + (tt * 7 + ks) * 2048), ld16(R, wbase + (tt * 7 + ks) * 2048 + 1024));
        dma16(R, wbase + (tt * 7 + 6) * 2048, ldsb + LA_W + (w * 7 + tt) * 2048);
        dma16(R, wbase + (tt * 7 + 6) * 2048 + 1024, ldsb + LA_W + (w * 7 + tt) * 2048 + 1024);
    }
    auto stage_x = [&](int s) {
        const unsigned src = OFF_XF + (s * 4 + w) * 8192 + lane * 16, dst = ldsb + LA_X + (s & 1) * 32768 + w * 8192;
#pragma unroll
        for (int q = 0; q < 8; ++q) dma16(R, src + q * 1024, dst + q * 1024);
    };
    stage_x(0);
    bf16x8 CB;
    {
        const int c = lane & 15, g = lane >> 4;
#pragma unroll
        for (int jj = 0; jj < 8; ++jj) {
            const bool one = (jj < 4) ? (c < 8 && ((4 * g + jj) & 7) == c) : (c >= 8 && ((4 * g + jj - 4) & 7) == c - 8);
            CB[jj] = one ? (__bf16)1.0f : (__bf16)0.0f;
        }
    }
    const unsigned idx = (lane & 8) ? 0xEEEEu : 0x4444u;
    const bool sel = lane & 8;
    const int g = lane >> 4, col = lane & 15;
    const unsigned swr = LA_S + (w * 8 * 52 + (4 * (g & 1) + (g >> 1)) * 52 + col) * 4;
    const int rr = lane >> 5, q = lane & 31;
    const int irow = 2 * w + rr;
    const unsigned srd = LA_S + (irow * 52 + ((q < 4) ? 32 + 2 * q : 2 * (q - 4) & 31)) * 4;
    const int piece = q >> 2, dw = q & 3;
    const int pn = (piece == 0) ? (h >> 3) : 2 + (h >> 1);
    const unsigned pin = (piece == 0) ? (h & 7) * 128 : (h & 1) * 512 + (piece - 1) * 128;
    const int pwin = (pn >= 5) ? 2 : 1;
    const unsigned poff = (pn % 5) * 1024 + pin + irow * 16 + dw * 4;
    wait_vm0();
    for (int s = 0; s < NSEG - 1; ++s) {
        v4u m[7];
        {
            if (!poll_frags<7, 7>(R, lane * 16, ex_win(posbits, 3 * s), 0, m)) return;
        }
        if (s + 1 < NSEG - 1) stage_x(s + 1);
        {
            const int i = lane & 7;
#pragma unroll
            for (int ks = 0; ks < 7; ++ks) {
                const int cl = 8 * ks + (lane >> 3) - 14 * w;
                if ((unsigned)cl < 14u) {
                    uchar* mbp = lds + LA_M + w * 4096 + i * 16;
                    if (cl < 8) *(v4u*)(mbp + (cl >> 2) * 1024 + (cl & 3) * 256) = m[ks];
                    else if (cl < 12) *(v4u*)(mbp + 2048 + ((cl - 8) >> 1) * 1024 + (2 * (cl & 1)) * 256) = m[ks];
                    else {
                        *(v4u*)(mbp + 2048 + (cl - 12) * 1024 + 1 * 256) = (v4u){m[ks].x, m[ks].y, 0u, 0u};
                        *(v4u*)(mbp + 2048 + (cl - 12) * 1024 + 3 * 256) = (v4u){m[ks].z, m[ks].w, 0u, 0u};
                    }
                }
            }
        }
        f32x4 acc[7];
#pragma unroll
        for (int tt = 0; tt < 7; ++tt) acc[tt] = (f32x4){0.f, 0.f, 0.f, 0.f};
#pragma unroll
        for (int ks = 0; ks < 6; ++ks)
#pragma unroll
            for (int tt = 0; tt < 7; ++tt) acc[tt] = SMF(asb8(m[ks]), W[tt][ks], acc[tt], idx);
#pragma unroll
        for (int tt = 0; tt < 7; ++tt) {
            const unsigned o = LA_W + (w * 7 + tt) * 2048 + lane * 16;
            acc[tt] = SMF(asb8(m[6]), cat16(lds_ld16(lds, o), lds_ld16(lds, o + 1024)), acc[tt], idx);
        }
        v2u dp[7];
#pragma unroll
        for (int tt = 0; tt < 7; ++tt) dp[tt] = (v2u){pk(acc[tt][0], acc[tt][1]), pk(acc[tt][2], acc[tt][3])};
        f32x4 tp[4];
#pragma unroll
        for (int p = 0; p < 4; ++p) {
            const v2u a = dp[2 * p], b = dp[(p < 3) ? 2 * p + 1 : 6];
            tp[p] = __builtin_amdgcn_mfma_f32_16x16x32_bf16(asb8((v4u){a.x, a.y, b.x, b.y}), CB, (f32x4){0.f, 0.f, 0.f, 0.f}, 0, 0, 0);
        }
        const v4u A0 = {pk(tp[0][0], tp[0][1]), pk(tp[0][2], tp[0][3]), pk(tp[1][0], tp[1][1]), pk(tp[1][2], tp[1][3])};
        const v4u A1 = {pk(tp[2][0], tp[2][1]), pk(tp[2][2], tp[2][3]), sel ? pk(tp[3][2], tp[3][3]) : pk(tp[3][0], tp[3][1]), 0u};
        f32x4 sa[3];
#pragma unroll
        for (int tau = 0; tau < 3; ++tau) {
            const unsigned b0 = (tau < 2) ? LA_X + (s & 1) * 32768 + w * 8192 + tau * 2048 + lane * 16 : LA_M + w * 4096 + lane * 16;
            const unsigned b1 = (tau < 2) ? b0 + 4096 : b0 + 2048;
            sa[tau] = SMF(asb8(A0), cat16(lds_ld16(lds, b0), lds_ld16(lds, b0 + 1024)), ((f32x4){0.f, 0.f, 0.f, 0.f}), idx);
            sa[tau] = SMF(asb8(A1), cat16(lds_ld16(lds, b1), lds_ld16(lds, b1 + 1024)), sa[tau], idx);
        }
        const unsigned sb = (s & 1) * 6656;
#pragma unroll
        for (int tau = 0; tau < 3; ++tau) {
            *(float*)(lds + swr + sb + tau * 64) = fold32(sa[tau][0], sa[tau][1]);
            *(float*)(lds + swr + sb + tau * 64 + 2 * 52 * 4) = fold32(sa[tau][2], sa[tau][3]);
        }
        raw_barrier();
        f32x2 v = *(const f32x2*)(lds + srd + sb);
#pragma unroll
        for (int ww = 1; ww < 4; ++ww) { const f32x2 u = *(const f32x2*)(lds + srd + sb + ww * 8 * 52 * 4); v += u; }
        if (q >= 20) v = (f32x2){-INFINITY, -INFINITY};
        const float mx = half_max(fmaxf(v.x, v.y));
        const float e0 = __builtin_amdgcn_exp2f(v.x - mx), e1 = __builtin_amdgcn_exp2f(v.y - mx);
        const float inv = __builtin_amdgcn_rcpf(half_sum(e0 + e1));
        const unsigned pv = min(pk(e0 * inv, e1 * inv), 0xFFFFFFFEu);
        if (q < 20) st4_sc1(R, ex_win(posbits, 3 * s + pwin) + poff, pv);
    }
}

constexpr unsigned LB_W = 0;
constexpr unsigned LB_Y = 86016;
constexpr unsigned LB_Z = LB_Y + 32768;
constexpr unsigned LB_END = LB_Z + 8192;
template <bool PV>
DEV void chain_B(rsrc_t R, uchar* lds, int b, int wv, unsigned posbits) {
    constexpr int NREG = PV ? 6 : 7;
    const int lane = threadIdx.x & 63;
    const unsigned ldsb = (unsigned)(uintptr_t)(LDSP uchar*)lds;
    bf16x16 W[NREG][7];
    const unsigned wbase = OFF_WFB + (b * 4 + wv) * (8 * 7 * 2048) + lane * 16;
    const unsigned wl = LB_W + (PV ? wv * 28672 : 57344 + (wv - 2) * 14336);
#pragma unroll
    for (int tl = 0; tl < NREG; ++tl)
#pragma unroll
        for (int ks = 0; ks < 7; ++ks) W[tl][ks] = cat16(ld16(R, wbase + (tl * 7 + ks) * 2048), ld16(R, wbase + (tl * 7 + ks) * 2048 + 1024));
#pragma unroll
    for (int q = 0; q < (8 - NREG) * 14; ++q) dma16(R, wbase + NREG * 14336 + q * 1024, ldsb + wl + q * 1024);
    auto stage_y = [&](int s) {
        const unsigned src = OFF_YX + (s * 14 + b) * 32768 + (wv - 2) * 16384 + lane * 16, dst = ldsb + LB_Y + (wv - 2) * 16384;
#pragma unroll
        for (int q = 0; q < 16; ++q) dma16(R, src + q * 1024, dst + q * 1024);
    };
    if (!PV) stage_y(0);
    const unsigned idx = (lane & 8) ? 0xEEEEu : 0x4444u;
    const int g = lane >> 4, c = lane & 15;
    wait_vm0();
    for (int s = 0; s < NSEG; ++s) {
        v4u m[7];
        {
            if (!poll_frags<7, 7>(R, lane * 16, ex_win(posbits, 3 * s), 0, m)) return;
        }
        f32x4 acc[8];
#pragma unroll
        for (int tl = 0; tl < 8; ++tl) acc[tl] = (f32x4){0.f, 0.f, 0.f, 0.f};
#pragma unroll
        for (int ks = 0; ks < 7; ++ks) {
#pragma unroll
            for (int tl = 0; tl < NREG; ++tl) acc[tl] = SMF(asb8(m[ks]), W[tl][ks], acc[tl], idx);
#pragma unroll
            for (int tl = NREG; tl < 8; ++tl) {
                const unsigned o = wl + ((tl - NREG) * 7 + ks) * 2048 + lane * 16;
                acc[tl] = SMF(asb8(m[ks]), cat16(lds_ld16(lds, o), lds_ld16(lds, o + 1024)), acc[tl], idx);
            }
        }
#pragma unroll
        for (int pr = 0; pr < 4; ++pr) {
            const f32x4 X = acc[2 * pr], Y = acc[2 * pr + 1];
            const unsigned d01 = pk(fold32(X[0], Y[0]), fold32(X[1], Y[1])), d23 = pk(fold32(X[2], Y[2]), fold32(X[3], Y[3]));
            const int ct = pr & 1, pair = 2 * wv + (pr >> 1), isY = g >> 1, g1 = g & 1;
            unsigned* zp = (unsigned*)(lds + LB_Z + (((ct * 8 + pair) * 2 + g1) * 16 + c) * 16 + isY * 4);
            zp[0] = d01; zp[2] = d23;
            st8(R, OFF_ZH + ((((s * 14 + b) * 2 + ct) * 16 + 2 * pair + isY) * 16 + c) * 16 + g1 * 8, (v2u){d01, d23});
        }
        if (s == NSEG - 1) break;
        if (!PV) wait_vm0();
        raw_barrier();
        if (PV) {
            const int ct = wv;
            bf16x16 V[10];
#pragma unroll
            for (int mm = 0; mm < 2; ++mm) {
                const unsigned o = LB_Z + (((ct * 8 + 4 * mm + (g >> 1)) * 2 + (g & 1)) * 16 + c) * 16;
                V[mm] = cat16(lds_ld16(lds, o), lds_ld16(lds, o + 2 * 512));
            }
#pragma unroll
            for (int n = 0; n < 8; ++n) {
                const unsigned o = LB_Y + (ct * 16 + 2 * n) * 1024 + lane * 16;
                V[2 + n] = cat16(lds_ld16(lds, o), lds_ld16(lds, o + 1024));
            }
#pragma unroll
            for (int n = 0; n < 10; ++n) asm volatile("" : "+v"(V[n]));
            raw_barrier();
            v4u P[10];
            {
                if (!poll_frags<10, 5>(R, lane * 16, ex_win(posbits, 3 * s + 1), ex_win(posbits, 3 * s + 2), P)) return;
            }
            f32x4 o = {0.f, 0.f, 0.f, 0.f};
#pragma unroll
            for (int n = 0; n < 10; ++n) o = SMF(asb8(P[n]), V[n], o, idx);
            const float U = fold32(o[0], o[2]), Vv = fold32(o[1], o[3]);
            const bool odd = lane & 1;
            const float snd = odd ? U : Vv;
            const float rcv = __builtin_bit_cast(float, __builtin_amdgcn_update_dpp(0, __builtin_bit_cast(int, snd), 0xB1, 0xf, 0xf, false));
            const unsigned val = min(odd ? pk(rcv, Vv) : pk(U, rcv), 0xFFFFFFFEu);
            const int i = 4 * (g & 1) + 2 * (g >> 1) + (c & 1);
            st4_sc1(R, ex_win(posbits, 3 * (s + 1)) + b * 512 + ct * 256 + (c >> 3) * 128 + i * 16 + ((c & 7) >> 1) * 4, val);
        } else {
            raw_barrier();
            if (s + 1 < NSEG - 1) stage_y(s + 1);
        }
    }
}

__global__ void __launch_bounds__(256, 1) k_chain(uchar* __restrict__ ws) {
    extern __shared__ __attribute__((aligned(16))) uchar lds[];
    if (blockIdx.x & 7) return;
    const int rank = blockIdx.x >> 3;
    if (rank >= 30) return;
    const rsrc_t R = mk_rsrc(ws, WS_USED);
    const unsigned posbits = 0;
    if (rank < 16) chain_A(R, lds, rank, posbits);
    else {
        const int wv = rfl(threadIdx.x >> 6);
        if (wv < 2) chain_B<true>(R, lds, rank - 16, wv, posbits);
        else chain_B<false>(R, lds, rank - 16, wv, posbits);
    }
}

__global__ void __launch_bounds__(256) k_xs(uchar* __restrict__ ws) {
    const rsrc_t R = mk_rsrc(ws, WS_USED);
    const int bid = blockIdx.x, tid = threadIdx.x, lane = tid & 63, wave = tid >> 6;
    const int s = bid >> 2, h = 4 * (bid & 3) + wave, r = lane & 31, hh = lane >> 5;
    const unsigned posbits = 0;
    f32x16 ax, am;
#pragma unroll
    for (int i = 0; i < 16; ++i) { ax[i] = 0.f; am[i] = 0.f; }
    const unsigned kx = OFF_XC + s * 28672 + hh * 512 + swap23(r) * 16;
    const unsigned km = ex_win(posbits, 3 * s) + hh * 128 + (r & 7) * 16;
    const unsigned tq = OFF_T + (s * 16 + h) * 28672 + lane * 16;
#pragma unroll 4
    for (int kk = 0; kk < 28; ++kk) {
        const bf16x8 bq = asb8(ld16(R, tq + kk * 1024));
        ax = __builtin_amdgcn_mfma_f32_32x32x16_bf16(asb8(ld16(R, kx + kk * 1024)), bq, ax, 0, 0, 0);
        am = __builtin_amdgcn_mfma_f32_32x32x16_bf16(asb8(ld16(R, km + kk * 256)), bq, am, 0, 0, 0);
    }
    float mx = fmaxf(fmaxf(am[0], am[1]), fmaxf(am[2], am[3]));
#pragma unroll
    for (int i = 0; i < 16; ++i) {
        const int key = 16 * (i >> 3) + 8 * hh + (i & 7);
        if (key > r) ax[i] = -INFINITY;
        mx = fmaxf(mx, ax[i]);
    }
    mx = fmaxf(mx, __shfl_xor(mx, 32));
    float sum = 0.f;
#pragma unroll
    for (int i = 0; i < 16; ++i) { ax[i] = __builtin_amdgcn_exp2f(ax[i] - mx); sum += ax[i]; }
#pragma unroll
    for (int i = 0; i < 4; ++i) { am[i] = __builtin_amdgcn_exp2f(am[i] - mx); sum += am[i]; }
    sum += __shfl_xor(sum, 32);
    const float inv = 1.0f / sum;
    const unsigned pb = OFF_PX + (s * 16 + h) * 2560 + r * 16;
    st8(R, pb + hh * 8, (v2u){pk(am[0] * inv, am[1] * inv), pk(am[2] * inv, am[3] * inv)});
#pragma unroll
    for (int x = 0; x < 2; ++x)
        st16(R, pb + (1 + 2 * x + hh) * 512,
             (v4u){pk(ax[8 * x] * inv, ax[8 * x + 1] * inv), pk(ax[8 * x + 2] * inv, ax[8 * x + 3] * inv), pk(ax[8 * x + 4] * inv, ax[8 * x + 5] * inv),
                   pk(ax[8 * x + 6] * inv, ax[8 * x + 7] * inv)});
}

__global__ void __launch_bounds__(256) k_xo(const __bf16* __restrict__ x, uchar* __restrict__ ws, __bf16* __restrict__ out) {
    __shared__ float red[4][16][64];
    const rsrc_t R = mk_rsrc(ws, WS_USED);
    const int bid = blockIdx.x, tid = threadIdx.x, lane = tid & 63, wave = tid >> 6;
    const int s = bid / 14, b = bid % 14, r = lane & 31, hh = lane >> 5;
    const int n5 = swap23(r), ct = n5 >> 4, c16 = kapinv16(n5 & 15);
    f32x16 acc;
#pragma unroll
    for (int i = 0; i < 16; ++i) acc[i] = 0.f;
#pragma unroll
    for (int hl = 0; hl < 4; ++hl) {
        const int h = 4 * wave + hl;
#pragma unroll
        for (int kk = 0; kk < 2; ++kk) {
            const bf16x8 a = asb8(ld16(R, OFF_YX + ((((s * 14 + b) * 2 + ct) * 16 + h) * 4 + 2 * kk + hh) * 256 + c16 * 16));
            const bf16x8 p = asb8(ld16(R, OFF_PX + (s * 16 + h) * 2560 + (1 + 2 * kk + hh) * 512 + r * 16));
            acc = __builtin_amdgcn_mfma_f32_32x32x16_bf16(a, p, acc, 0, 0, 0);
        }
    }
#pragma unroll
    for (int mm = 0; mm < 2; ++mm) {
        const int h = 4 * wave + 2 * mm + hh;
        const bf16x8 a = asb8(ld16(R, OFF_ZH + (((s * 14 + b) * 2 + ct) * 16 + h) * 256 + c16 * 16));
        const bf16x8 p = asb8(ld16(R, OFF_PX + (s * 16 + h) * 2560 + r * 16));
        acc = __builtin_amdgcn_mfma_f32_32x32x16_bf16(a, p, acc, 0, 0, 0);
    }
#pragma unroll 4
    for (int k = 0; k < 28; ++k) {
        const int kk = 28 * wave + k;
        const bf16x8 a = asb8(ld16(R, OFF_W2F + (b * 112 + kk) * 1024 + lane * 16));
        const bf16x8 p = asb8(ld16(R, OFF_HID + (s * 224 + 2 * kk) * 512 + lane * 16));
        acc = __builtin_amdgcn_mfma_f32_32x32x16_bf16(a, p, acc, 0, 0, 0);
    }
#pragma unroll
    for (int i = 0; i < 16; ++i) red[wave][i][lane] = acc[i];
    __syncthreads();
    if (wave < 2) {
        const int o = (32 * s + r) * 448 + 32 * b + 16 * wave + 8 * hh;
        const v4u xv = *(const v4u*)(x + o);
        float f[8] = {bflo(xv.x), bfhi(xv.x), bflo(xv.y), bfhi(xv.y), bflo(xv.z), bfhi(xv.z), bflo(xv.w), bfhi(xv.w)};
#pragma unroll
        for (int i = 0; i < 8; ++i) f[i] += (red[0][8 * wave + i][lane] + red[1][8 * wave + i][lane]) + (red[2][8 * wave + i][lane] + red[3][8 * wave + i][lane]);
        *(v4u*)(out + o) = (v4u){pk(f[0], f[1]), pk(f[2], f[3]), pk(f[4], f[5]), pk(f[6], f[7])};
    }
}

extern "C" void kernel_launch(void* const* d_in, const int* in_sizes, int n_in, void* d_out, int out_size, void* d_ws, size_t ws_size, hipStream_t stream) {
    const __bf16* x = (const __bf16*)d_in[0];
    const __bf16* mem0 = (const __bf16*)d_in[1];
    const __bf16* wq = (const __bf16*)d_in[2];
    const __bf16* wk = (const __bf16*)d_in[3];
    const __bf16* wv = (const __bf16*)d_in[4];
    const __bf16* wo = (const __bf16*)d_in[5];
    const __bf16* w1 = (const __bf16*)d_in[8];
    const __bf16* w2 = (const __bf16*)d_in[9];
    uchar* ws = (uchar*)(((uintptr_t)d_ws + 524287u) & ~(uintptr_t)524287u);
    (void)hipFuncSetAttribute((const void*)k_bulk, hipFuncAttributeMaxDynamicSharedMemorySize, 114688);
    (void)hipFuncSetAttribute((const void*)k_chain, hipFuncAttributeMaxDynamicSharedMemorySize, 160 * 1024);
    k_prep<<<1296 + 896, 256, 0, stream>>>(x, w1, w2, wo, ws);
    k_wprod<<<224 + 192, 256, 0, stream>>>(wq, wk, wv, mem0, ws);
    k_bulk<<<252, 256, 114688, stream>>>(ws);
    k_chain<<<256, 256, 160 * 1024, stream>>>(ws);
    k_xs<<<256, 256, 0, stream>>>(ws);
    k_xo<<<896, 256, 0, stream>>>(x, ws, (__bf16*)d_out);
}
```

```cpp
#define NW2 192
#define XMAP 0
#define PFW 1
#define BFULL 1
#define BFULL2 1
#define WDELAY 0
#define FREE01 1
#define NXS 42
#define EX_NM 2
#define EX_NP 2
#define D_A0 58
#define D_B0 84
#define D_Z0 200
#define D_UP 0
#define D_DN 0
#include <hip/hip_runtime.h>
#include <stdint.h>

typedef __bf16 bf16x2 __attribute__((ext_vector_type(2)));
typedef __bf16 bf16x8 __attribute__((ext_vector_type(8)));
typedef __bf16 bf16x16 __attribute__((ext_vector_type(16)));
typedef float f32x2 __attribute__((ext_vector_type(2)));
typedef float f32x4 __attribute__((ext_vector_type(4)));
typedef float f32x16 __attribute__((ext_vector_type(16)));
typedef unsigned v2u __attribute__((ext_vector_type(2)));
typedef unsigned v4u __attribute__((ext_vector_type(4)));
typedef unsigned v8u __attribute__((ext_vector_type(8)));
typedef unsigned char uchar;
typedef __amdgpu_buffer_rsrc_t rsrc_t;
#define DEV __device__ __forceinline__
#define LDSP __attribute__((address_space(3)))

constexpr int NSEG = 64;
constexpr unsigned SENT = 0xFFFFFFFFu;
constexpr unsigned SPIN_LIMIT = 1u << 17;
constexpr int NREG_CAL = 6;

constexpr unsigned OFF_EX = 0;
constexpr unsigned OFF_XA = OFF_EX + (3u << 20);
constexpr unsigned OFF_XC = OFF_XA + 1835008u;
constexpr unsigned OFF_XF = OFF_XC + 1835008u;
constexpr unsigned OFF_WFA = OFF_XF + 2097152u;
constexpr unsigned OFF_WFB = OFF_WFA + 6422528u;
constexpr unsigned OFF_W1F = OFF_WFB + 6422528u;
constexpr unsigned OFF_W2F = OFF_W1F + 1605632u;
constexpr unsigned OFF_WOT = OFF_W2F + 1605632u;
constexpr unsigned OFF_T = OFF_WOT + 3670016u;
#define YPAD 0
constexpr unsigned YB = 32768u + YPAD;
constexpr unsigned OFF_YX = OFF_T + 29360128u;
constexpr unsigned OFF_HID = OFF_YX + 896u * YB;
constexpr unsigned OFF_ZH = OFF_HID + 7340032u;
constexpr unsigned OFF_PX = OFF_ZH + 7340032u;
constexpr unsigned OFF_CTL = OFF_PX + 2621440u;
constexpr unsigned CTL_ZP = OFF_CTL + 1024u;
constexpr unsigned CTL_YP = OFF_CTL + 2048u;
constexpr unsigned CTL_BD = OFF_CTL + 4096u;
constexpr unsigned CTL_PF = OFF_CTL + 8192u;
constexpr unsigned CTL_END = OFF_CTL + 16384u;
constexpr unsigned OFF_TS = CTL_END;
constexpr unsigned OFF_RT = OFF_TS + 30u * 8192u;
constexpr unsigned WS_USED = OFF_RT + 256u * 128u;

DEV rsrc_t mk_rsrc(const void* p, unsigned bytes) { return __builtin_amdgcn_make_buffer_rsrc((void*)p, 0, bytes, 0x00020000); }
DEV v4u ld16(rsrc_t r, unsigned off) { return __builtin_amdgcn_raw_buffer_load_b128(r, off, 0, 0); }
DEV v4u ld16s(rsrc_t r, unsigned off, unsigned soff) { return __builtin_amdgcn_raw_buffer_load_b128(r, off, soff, 0); }
DEV v4u ld16c(rsrc_t r, unsigned off) { return __builtin_amdgcn_raw_buffer_load_b128(r, off, 0, 16); }
DEV void st8_sc1(rsrc_t r, unsigned off, v2u v) { __builtin_amdgcn_raw_buffer_store_b64(v, r, off, 0, 16); }
DEV unsigned ld4_poll(rsrc_t r, unsigned off) {
    unsigned v;
    asm volatile("buffer_load_dword %0, %1, %2, 0 offen sc1\n\ts_waitcnt vmcnt(0)" : "=v"(v) : "v"(off), "s"(r) : "memory");
    return v;
}
DEV v4u ld16_sc1(rsrc_t r, unsigned off, unsigned soff) { return __builtin_amdgcn_raw_buffer_load_b128(r, off, soff, 16); }
DEV void st16(rsrc_t r, unsigned off, v4u v) { __builtin_amdgcn_raw_buffer_store_b128(v, r, off, 0, 0); }
DEV void st8(rsrc_t r, unsigned off, v2u v) { __builtin_amdgcn_raw_buffer_store_b64(v, r, off, 0, 0); }
DEV void st4(rsrc_t r, unsigned off, unsigned v) { __builtin_amdgcn_raw_buffer_store_b32(v, r, off, 0, 0); }
DEV void st4_sc1(rsrc_t r, unsigned off, unsigned soff, unsigned v) { __builtin_amdgcn_raw_buffer_store_b32(v, r, off, soff, 16); }
DEV void st16_sc1(rsrc_t r, unsigned off, v4u v) { __builtin_amdgcn_raw_buffer_store_b128(v, r, off, 0, 16); }
DEV unsigned pk(float a, float b) { bf16x2 v = {(__bf16)a, (__bf16)b}; return __builtin_bit_cast(unsigned, v); }
DEV float asf(unsigned u) { return __builtin_bit_cast(float, u); }
DEV float bflo(unsigned u) { return __builtin_bit_cast(float, u << 16); }
DEV float bfhi(unsigned u) { return __builtin_bit_cast(float, u & 0xFFFF0000u); }
DEV int swap23(int r) { return (r & 0x13) | ((r & 4) << 1) | ((r & 8) >> 1); }
DEV int perm5(int r) { return 16 * (r & 1) + 4 * ((r >> 1) & 1) + 2 * ((r >> 2) & 1) + ((r >> 3) & 1) + 8 * ((r >> 4) & 1); }
DEV int colx(int chi, int j) { return 32 * (chi >> 2) + 4 * (chi & 3) + 16 * (j & 1) + (j >> 1); }
DEV int colp(int dc, int e) { return 32 * (dc >> 2) + 8 * ((dc >> 1) & 1) + 2 * (dc & 1) + 16 * (e & 1) + 4 * ((e >> 1) & 1) + (e >> 2); }
DEV bf16x8 asb8(v4u v) { return __builtin_bit_cast(bf16x8, v); }
DEV bf16x16 cat16(v4u a, v4u b) { v8u r = {a.x, a.y, a.z, a.w, b.x, b.y, b.z, b.w}; return __builtin_bit_cast(bf16x16, r); }
DEV void wait_vm0() { asm volatile("s_waitcnt vmcnt(0)" ::: "memory"); }
DEV void wait_lgkm0() { asm volatile("s_waitcnt lgkmcnt(0)" ::: "memory"); }
DEV void raw_barrier() { asm volatile("s_waitcnt lgkmcnt(0)\n\ts_barrier" ::: "memory"); }
DEV void dma16(rsrc_t r, unsigned voff, unsigned ldsbase) {
    asm volatile("s_mov_b32 m0, %0\n\ts_nop 0\n\tbuffer_load_dwordx4 %1, %2, 0 offen lds" ::"s"(ldsbase), "v"(voff), "s"(r) : "m0", "memory");
}
DEV unsigned rfl(unsigned v) { return __builtin_amdgcn_readfirstlane(v); }
DEV v4u lds_ld16(const uchar* lds, unsigned off) { return *(const v4u*)(lds + off); }
DEV unsigned ex_chunk(unsigned posbits, int c) { return OFF_EX + (unsigned)(c >> 1) * 32768u + ((posbits >> (4 * (c >> 5) + 2 * (c & 1))) & 3u) * 8192u; }
DEV unsigned ld_posbits(const uchar* ws) {
    const unsigned* t = (const unsigned*)(ws + OFF_CTL);
    unsigned p = 0;
#pragma unroll
    for (int r = 0; r < NREG_CAL; ++r) p |= (t[r] & 15u) << (4 * r);
    return rfl(p);
}
DEV unsigned umax3(unsigned a, unsigned b, unsigned c) { return max(max(a, b), c); }
DEV int lane_id() { int l; asm volatile("v_mbcnt_lo_u32_b32 %0, -1, 0\n\tv_mbcnt_hi_u32_b32 %0, -1, %0" : "=v"(l)); return l; }
#define RT_STAMP(slot) do { } while (0)
DEV unsigned clk32() { return (unsigned)__builtin_amdgcn_s_memtime(); }

#define PROBE_VAL(par) ((par) | (((par) + 2u) << 2))
#define PROBE_REPS 4
typedef __bf16 bf16x4 __attribute__((ext_vector_type(4)));
DEV int feat_col(int w, int tt, int n) {
    int jp, bq, val;
    if (tt < 6) {
        const int cs = 8 * (tt >> 2) + 2 * (n >> 2) + ((tt >> 1) & 1);
        val = 4 * ((n >> 1) & 1) + 2 * (tt & 1) + (n & 1);
        if (cs < 8) { jp = cs >> 1; bq = cs & 1; } else { jp = 4 + ((cs - 8) >> 2); bq = ((cs - 8) >> 1) & 1; }
    } else { const int k = n >> 2; jp = 6; bq = k >> 1; val = 4 * (k & 1) + (n & 3); }
    return colx(8 * ((jp + 2 * w) % 7) + 2 * w + bq, val);
}
DEV bf16x4 lds_tr(const uchar* p) { return __builtin_amdgcn_ds_read_tr16_b64_v4bf16((LDSP bf16x4*)(uintptr_t)(unsigned)(uintptr_t)(LDSP const uchar*)p); }
constexpr unsigned PREP_LDS = 147456;
constexpr int PREP_THREADS = 768;
#define PST16 st16_sc1
#define PRE_ST 8
#define PSTX st16_sc1
#define PREP_STAMP(slot) do { } while (0)
template <int TYPE>
DEV void prod_half(const uchar* lds, int wave, int lane, f32x16 (&acc)[2]) {
    const int r = lane & 31, hh = lane >> 5;
    bf16x8 B[16];
    if (TYPE == 0) {
        const uchar* pb = lds + 32768 + wave * 16384 + r * 512;
#pragma unroll
        for (int kk = 0; kk < 16; ++kk) B[kk] = *(const bf16x8*)(pb + (((2 * kk + hh) ^ (r & 15)) * 16));
    } else {
        const int blk = (lane >> 4) & 1, q = (lane & 15) >> 2, p = lane & 3;
        const uchar* pb = lds + 32768 + wave * 16384 + (8 * hh + q) * 64 + (2 * blk + (p >> 1)) * 16 + 8 * (p & 1);
#pragma unroll
        for (int kk = 0; kk < 16; ++kk) {
            const bf16x4 lo = lds_tr(pb + kk * 1024), hi = lds_tr(pb + kk * 1024 + 256);
            B[kk] = __builtin_shufflevector(lo, hi, 0, 1, 2, 3, 4, 5, 6, 7);
        }
    }
#pragma unroll
    for (int dt = 0; dt < 2; ++dt)
#pragma unroll
        for (int i = 0; i < 16; ++i) acc[dt][i] = 0.f;
#pragma unroll
    for (int kk = 0; kk < 16; ++kk)
#pragma unroll
        for (int dt = 0; dt < 2; ++dt) {
            const bf16x8 a = *(const bf16x8*)(lds + (32 * dt + r) * 512 + (((2 * kk + hh) ^ (r & 15)) * 16));
            acc[dt] = __builtin_amdgcn_mfma_f32_32x32x16_bf16(a, B[kk], acc[dt], 0, 0, 0);
        }
}
DEV void prod_store(rsrc_t R, int type, int h, int ds, int ft, int lane, const f32x16 (&acc)[2]) {
    const float scale = type ? 1.0f : 0.09016844005556021f;
    const int r = lane & 31, hh = lane >> 5;
    unsigned dst;
    if (type == 0) {
        const int q = 2 * ft + (r >> 4), w = q / 7, tt = q % 7;
        const int jin = (ds - 2 * w + 7) % 7;
        dst = OFF_WFA + (((h * 4 + w) * 7 + jin) * 7 + tt) * 2048 + (r & 15) * 16;
    } else dst = OFF_WFB + ((ft * 32 + 2 * h + (r >> 4)) * 7 + ds) * 2048 + (r & 15) * 16;
#pragma unroll
    for (int dt = 0; dt < 2; ++dt)
#pragma unroll
        for (int xx = 0; xx < 2; ++xx) {
            const f32x16& a = acc[dt];
            v4u o = {pk(a[8 * xx] * scale, a[8 * xx + 1] * scale), pk(a[8 * xx + 2] * scale, a[8 * xx + 3] * scale),
                     pk(a[8 * xx + 4] * scale, a[8 * xx + 5] * scale), pk(a[8 * xx + 6] * scale, a[8 * xx + 7] * scale)};
            PST16(R, dst + dt * 1024 + (2 * xx + hh) * 256, o);
        }
}
__global__ void __launch_bounds__(PREP_THREADS, 3) k_prep(const __bf16* __restrict__ x, const __bf16* __restrict__ wq, const __bf16* __restrict__ wk,
                                                          const __bf16* __restrict__ wv, const __bf16* __restrict__ wo, const __bf16* __restrict__ w1,
                                                          const __bf16* __restrict__ w2, uchar* __restrict__ ws) {
    extern __shared__ __attribute__((aligned(16))) uchar lds[];
    const int lane = threadIdx.x & 63, wave = rfl(threadIdx.x >> 6);
    const int xq = blockIdx.x & 7, rank = blockIdx.x >> 3;
    const unsigned ldsb = (unsigned)(uintptr_t)(LDSP uchar*)lds;
    const rsrc_t R = mk_rsrc(ws, WS_USED);
    if (wave == 0) PREP_STAMP(16);
    if (wave >= 7) {
        __builtin_amdgcn_s_barrier();
        if (wave == 7) {
            const int r8 = lane & 7, cg = lane >> 3, row = blockIdx.x * 8 + r8, s = row >> 5, r5 = row & 31;
            v4u v[7];
#pragma unroll
            for (int j = 0; j < 7; ++j) v[j] = *(const v4u*)(x + row * 448 + (cg + 8 * j) * 8);
            float f[7][8];
            float sum = 0.f;
#pragma unroll
            for (int j = 0; j < 7; ++j) {
                f[j][0] = bflo(v[j].x); f[j][1] = bfhi(v[j].x); f[j][2] = bflo(v[j].y); f[j][3] = bfhi(v[j].y);
                f[j][4] = bflo(v[j].z); f[j][5] = bfhi(v[j].z); f[j][6] = bflo(v[j].w); f[j][7] = bfhi(v[j].w);
#pragma unroll
                for (int i = 0; i < 8; ++i) sum += f[j][i];
            }
            for (int o = 8; o < 64; o <<= 1) sum += __shfl_xor(sum, o);
            const float mean = sum * (1.0f / 448.0f);
            float sq = 0.f;
#pragma unroll
            for (int j = 0; j < 7; ++j)
#pragma unroll
                for (int i = 0; i < 8; ++i) { f[j][i] -= mean; sq += f[j][i] * f[j][i]; }
            for (int o = 8; o < 64; o <<= 1) sq += __shfl_xor(sq, o);
            const float rstd = 1.0f / sqrtf(sq * (1.0f / 448.0f) + 1e-5f);
            const int pos = cg & 3;
            const bool ah = pos < 2;
            const int tau = r5 >> 4, n = r5 & 15;
#pragma unroll
            for (int j = 0; j < 7; ++j) {
                float g[8];
#pragma unroll
                for (int i = 0; i < 8; ++i) g[i] = f[j][i] * rstd;
                v4u pc, pp;
                {
                    float rc[4];
                    for (int k = 0; k < 4; ++k) rc[k] = __shfl_xor(ah ? g[4 + k] : g[k], 16);
                    if (ah) pc = (v4u){pk(g[0], rc[0]), pk(g[1], rc[1]), pk(g[2], rc[2]), pk(g[3], rc[3])};
                    else pc = (v4u){pk(rc[0], g[4]), pk(rc[1], g[5]), pk(rc[2], g[6]), pk(rc[3], g[7])};
                }
                {
                    const int si[4] = {0, 1, 4, 5};
                    float rc[4];
                    for (int k = 0; k < 4; ++k) rc[k] = __shfl_xor(ah ? g[si[k] + 2] : g[si[k]], 16);
                    if (ah) pp = (v4u){pk(g[0], rc[0]), pk(g[4], rc[2]), pk(g[1], rc[1]), pk(g[5], rc[3])};
                    else pp = (v4u){pk(rc[0], g[2]), pk(rc[2], g[6]), pk(rc[1], g[3]), pk(rc[3], g[7])};
                }
                const int blk = (cg >> 2) + 2 * j;
                const int chi = 4 * blk + 2 * (pos & 1) + (pos >> 1), dc = chi;
                PSTX(R, OFF_XA + s * 28672 + (dc >> 1) * 1024 + ((dc & 1) * 32 + r5) * 16, pp);
                PSTX(R, OFF_XC + s * 28672 + chi * 512 + r5 * 16, pc);
                const int w = (chi >> 1) & 3, bq = chi & 1, jp = ((chi >> 3) - 2 * w + 7) % 7;
                const unsigned base = OFF_XF + (s * 4 + w) * 8192 + tau * 4096 + n * 16;
                if (jp < 6) {
                    const int cs = (jp < 4) ? 2 * jp + bq : 8 + 4 * (jp - 4) + 2 * bq;
                    PSTX(R, base + (cs >> 2) * 1024 + (cs & 3) * 256, pc);
                } else {
                    const int c0 = 9 + 4 * bq, c1 = 11 + 4 * bq;
                    PSTX(R, base + (c0 >> 2) * 1024 + (c0 & 3) * 256, ((v4u){pc.x, pc.y, 0u, 0u}));
                    PSTX(R, base + (c1 >> 2) * 1024 + (c1 & 3) * 256, ((v4u){pc.z, pc.w, 0u, 0u}));
                }
            }
            PREP_STAMP(17);
            return;
        }
        const int g = blockIdx.x * 4 + wave - 8;
        unsigned e1[2][8], e2[2][8];
#pragma unroll
        for (int it = 0; it < 2; ++it) {
            const int gi = min(g + 1024 * it, 1567);
            {
                const int fb = gi / 28, kk = gi % 28;
                const int f = 32 * fb + swap23(lane & 31), dc = 2 * kk + (lane >> 5);
                const unsigned short* src = (const unsigned short*)w1 + f;
#pragma unroll
                for (int j = 0; j < 8; ++j) e1[it][j] = src[colp(dc, j) * 1792];
            }
            {
                const int b = gi / 112, kk = gi % 112;
                const int n = 32 * b + swap23(lane & 31), f0 = 16 * kk + 8 * (lane >> 5);
                const unsigned short* src = (const unsigned short*)w2 + f0 * 448 + n;
#pragma unroll
                for (int j = 0; j < 8; ++j) e2[it][j] = src[j * 448];
            }
        }
        for (unsigned q = g * 64 + lane; q < NREG_CAL * 32768u; q += 65536u) PST16(R, OFF_EX + q * 16, ((v4u){SENT, SENT, SENT, SENT}));
        if (blockIdx.x == 248) for (int q = (wave - 8) * 64 + lane; q < (int)((CTL_END - CTL_ZP) / 16); q += 256) PST16(R, CTL_ZP + q * 16, ((v4u){0u, 0u, 0u, 0u}));
#pragma unroll
        for (int it = 0; it < 2; ++it) {
            const int gi = g + 1024 * it;
            if (gi < 1568) {
                const unsigned* a = e1[it];
                const unsigned* b = e2[it];
                PST16(R, OFF_W1F + (gi * 64 + lane) * 16, ((v4u){a[0] | (a[1] << 16), a[2] | (a[3] << 16), a[4] | (a[5] << 16), a[6] | (a[7] << 16)}));
                PST16(R, OFF_W2F + (gi * 64 + lane) * 16, ((v4u){b[0] | (b[1] << 16), b[2] | (b[3] << 16), b[4] | (b[5] << 16), b[6] | (b[7] << 16)}));
            }
        }
        if (wave == 8) PREP_STAMP(27);
        return;
    }
    if (xq == 0) {
        __builtin_amdgcn_s_barrier();
        if (rank >= NREG_CAL || wave >= 4) return;
        unsigned best = 1u << 30;
        for (int rep = 0; rep < PROBE_REPS; ++rep) {
            const unsigned off = OFF_EX + rank * 524288 + wave * 8192 + 7168 + rep * 128;
            if (lane == 0) st16_sc1(R, off, (v4u){SENT, SENT, SENT, SENT});
            wait_vm0();
            const unsigned t1 = clk32();
            v4u pv = ld16_sc1(R, off, 0);
            asm volatile("s_waitcnt vmcnt(0)" : "+v"(pv)::"memory");
            const unsigned t2 = clk32();
            best = min(best, t2 - t1);
        }
        if (lane == 0) *(unsigned*)(lds + wave * 4) = best;
        raw_barrier();
        if (wave == 0 && lane == 0) {
            const unsigned* bs = (const unsigned*)lds;
            const unsigned par = (min(bs[0], bs[2]) <= min(bs[1], bs[3])) ? 0u : 1u;
            *(unsigned*)(ws + OFF_CTL + rank * 4) = PROBE_VAL(par);
        }
        if (wave == 0) PREP_STAMP(20);
        return;
    }
    const int L = (xq - 1) * 32 + rank, grp = L / 7, ds = L - 7 * grp, type = grp >> 4, h = grp & 15;
    const rsrc_t RA = mk_rsrc(type ? wv : wq, 3670016u), RB = mk_rsrc(type ? wo : wk, 3670016u);
    const int hi = lane >> 5;
#pragma unroll
    for (int m = 0; m < 5; ++m) {
        const int j = wave + 7 * m;
        if (j < 32) {
            const int i = 2 * j + hi;
            dma16(RA, (64 * ds + 32 * (i >> 5) + perm5(i & 31)) * 8192 + h * 512 + (((lane & 31) ^ (i & 15)) * 16), ldsb + j * 1024);
        }
    }
    const int f0 = 2 * ds + wave, ft0 = f0 >= 14 ? f0 - 14 : f0, ft1 = ft0 >= 7 ? ft0 - 7 : ft0 + 7;
    const unsigned lb = ldsb + 32768 + wave * 16384;
    v4u st[16];
    const int rot = (wave & 1) ? 5 * (wave >> 1) : 4 * (wave >> 1);
    const unsigned lo0 = hi * (16 * 8192) + h * 512, lx = (lane & 31) ^ hi;
    const unsigned lo1 = (h * 256 + (lane >> 2)) * 896 + (lane & 3) * 16;
    unsigned sov = 0;
    if (type == 0) {
        const int i = lane & 15, q = 2 * ((lane & 16) ? ft1 : ft0) + (i >> 3);
        sov = feat_col(q / 7, q % 7, (2 * i) & 15) * 8192;
#pragma unroll
        for (int m = 0; m < 16; ++m) {
            const int i = (m + rot) & 15;
            dma16(RB, lo0 + ((lx ^ ((2 * i) & 15)) * 16) + __builtin_amdgcn_readlane(sov, i), lb + i * 1024);
        }
#pragma unroll
        for (int m = 0; m < PRE_ST; ++m) st[m] = ld16s(RB, lo0 + ((lx ^ ((2 * m) & 15)) * 16), __builtin_amdgcn_readlane(sov, 16 + m));
    } else {
#pragma unroll
        for (int m = 0; m < 16; ++m) dma16(RB, lo1 + ((m + rot) & 15) * (16 * 896) + ft0 * 64, lb + ((m + rot) & 15) * 1024);
#pragma unroll
        for (int m = 0; m < PRE_ST; ++m) st[m] = ld16s(RB, lo1, m * (16 * 896) + ft1 * 64);
    }
    asm volatile("s_waitcnt vmcnt(%0)\n\ts_barrier" ::"n"(PRE_ST) : "memory");
    if (type == 0) {
#pragma unroll
        for (int m = PRE_ST; m < 16; ++m) st[m] = ld16s(RB, lo0 + ((lx ^ ((2 * m) & 15)) * 16), __builtin_amdgcn_readlane(sov, 16 + m));
    } else {
#pragma unroll
        for (int m = PRE_ST; m < 16; ++m) st[m] = ld16s(RB, lo1, m * (16 * 896) + ft1 * 64);
    }
    if (wave == 0) PREP_STAMP(18);
    f32x16 acc[2];
    if (type == 0) prod_half<0>(lds, wave, lane, acc); else prod_half<1>(lds, wave, lane, acc);
    prod_store(R, type, h, ds, ft0, lane, acc);
    if (wave == 0) PREP_STAMP(19);
#pragma unroll
    for (int m = 0; m < 16; ++m) *(v4u*)(lds + 32768 + wave * 16384 + m * 1024 + lane * 16) = st[m];
    if (type == 0) prod_half<0>(lds, wave, lane, acc); else prod_half<1>(lds, wave, lane, acc);
    prod_store(R, type, h, ds, ft1, lane, acc);
    if (wave == 0) PREP_STAMP(20);
}

DEV unsigned wfa_piece(int h, int phi, int dc) {
    const int chi = phi >> 3, val = phi & 7;
    const int w = (chi >> 1) & 3, bq = chi & 1, jp = ((chi >> 3) - 2 * w + 7) % 7;
    int tt, nl;
    if (jp < 6) {
        const int cs = (jp < 4) ? 2 * jp + bq : 8 + 4 * (jp - 4) + 2 * bq;
        tt = 4 * (cs >> 3) + 2 * (cs & 1) + ((val >> 1) & 1);
        nl = 4 * ((cs >> 1) & 3) + 2 * (val >> 2) + (val & 1);
    } else { tt = 6; nl = 4 * (2 * bq + (val >> 2)) + (val & 3); }
    const int jin = ((dc >> 3) - 2 * w + 7) % 7;
    return OFF_WFA + (((h * 4 + w) * 7 + jin) * 7 + tt) * 2048 + ((dc >> 2) & 1) * 1024 + ((dc & 3) * 16 + nl) * 16;
}
#define BAHEAD 8
template <int TYPE>
DEV void bulk_task(rsrc_t R, uchar* lds, int wave, int h, int cb, int it0, int it1) {
    const int lane = lane_id();
    const int sg = wave >> 1, ch = wave & 1, r = lane & 31, hh = lane >> 5;
    const unsigned ldsb = (unsigned)(uintptr_t)(LDSP uchar*)lds;
    auto stage = [&](int it) {
        const unsigned src = OFF_XA + it * 57344 + wave * 14336 + lane * 16;
        const unsigned dst = ldsb + (it & 1) * 57344 + wave * 14336;
#pragma unroll
        for (int q = 0; q < 14; ++q) dma16(R, src + q * 1024, dst + q * 1024);
    };
    stage(it0);
    bf16x8 W[28];
#pragma unroll
    for (int kk = 0; kk < 28; ++kk) {
        unsigned off;
        const int dc = 2 * kk + hh;
        if (TYPE == 0) off = wfa_piece(h, 64 * cb + 32 * ch + swap23(r), dc);
        else if (TYPE == 1) {
            const int b = 2 * cb + ch, ct = r >> 4, nl = r & 15;
            off = OFF_WFB + ((b * 32 + 2 * h + ct) * 7 + (dc >> 3)) * 2048 + ((dc >> 2) & 1) * 1024 + ((dc & 3) * 16 + nl) * 16;
        } else off = OFF_W1F + ((2 * cb + ch) * 28 + kk) * 1024 + lane * 16;
        W[kk] = asb8(ld16(R, off));
    }
    const unsigned rdoff = sg * 28672 + ((TYPE == 1) ? (hh * 32 + perm5(r)) * 16 : lane * 16);
    for (int it = it0; it < it1; ++it) {
        const bool full = it - it0 < (TYPE == 1 ? BFULL : BFULL2);
        if (full) wait_vm0();
        else asm volatile("s_waitcnt vmcnt(2)" ::: "memory");
        raw_barrier();
        if (TYPE == 1 && wave == 0 && lane == 0) st4_sc1(R, CTL_YP + cb * 128 + h * 4, 0, full ? it : it - 1);
        if (it + 1 < it1) stage(it + 1);
        const uchar* xb = lds + (it & 1) * 57344 + rdoff;
        f32x16 acc;
#pragma unroll
        for (int i = 0; i < 16; ++i) acc[i] = 0.f;
        bf16x8 xf[28];
#pragma unroll
        for (int kk = 0; kk < BAHEAD; ++kk) xf[kk] = *(const bf16x8*)(xb + kk * 1024);
        __builtin_amdgcn_sched_barrier(0);
#pragma unroll
        for (int kk = 0; kk < 28; ++kk) {
            if (TYPE == 1) acc = __builtin_amdgcn_mfma_f32_32x32x16_bf16(xf[kk], W[kk], acc, 0, 0, 0);
            else acc = __builtin_amdgcn_mfma_f32_32x32x16_bf16(W[kk], xf[kk], acc, 0, 0, 0);
            if (kk + BAHEAD < 28) xf[kk + BAHEAD] = *(const bf16x8*)(xb + (kk + BAHEAD) * 1024);
            __builtin_amdgcn_sched_barrier(0);
        }
        const int s = 2 * it + sg;
#pragma unroll
        for (int x = 0; x < 2; ++x) {
            float a[8];
#pragma unroll
            for (int i = 0; i < 8; ++i) a[i] = (TYPE == 2) ? fmaxf(acc[8 * x + i], 0.f) : acc[8 * x + i];
            const v4u o = {pk(a[0], a[1]), pk(a[2], a[3]), pk(a[4], a[5]), pk(a[6], a[7])};
            unsigned off;
            if (TYPE == 0) off = OFF_T + ((s * 16 + h) * 56 + 8 * cb + 4 * ch + 2 * x + hh) * 512 + r * 16;
            else if (TYPE == 1) off = OFF_YX + (s * 14 + 2 * cb + ch) * YB + (((r >> 4) * 16 + h) * 4 + 2 * x + hh) * 256 + (r & 15) * 16;
            else off = OFF_HID + (s * 224 + 8 * cb + 4 * ch + 2 * x + hh) * 512 + r * 16;
            st16_sc1(R, off, o);
        }
    }
    wait_vm0();
    raw_barrier();
    if (TYPE == 1 && wave == 0 && lane == 0) st4_sc1(R, CTL_YP + cb * 128 + h * 4, 0, it1);
}

#define PRM_ARG
#define PRM_PASS
#define PRM_HOST
#define P_A0 D_A0
#define P_B0 D_B0
#define P_Z0 D_Z0
#define ZPV 280
#define P_UP D_UP
#define P_DN D_DN
#define ASM_CHAIN_A \
    "s_nop 7\n\t" \
    "s_mov_b32 s42, s13\n\t" \
    "buffer_load_dwordx4 a[0:3], v0, s[8:11], s42 offen\n\t" \
    "buffer_load_dwordx4 a[4:7], v0, s[8:11], s42 offen offset:1024\n\t" \
    "s_add_u32 s42, s42, 2048\n\t" \
    "buffer_load_dwordx4 a[8:11], v0, s[8:11], s42 offen\n\t" \
    "buffer_load_dwordx4 a[12:15], v0, s[8:11], s42 offen offset:1024\n\t" \
    "s_add_u32 s42, s42, 2048\n\t" \
    "buffer_load_dwordx4 a[16:19], v0, s[8:11], s42 offen\n\t" \
    "buffer_load_dwordx4 a[20:23], v0, s[8:11], s42 offen offset:1024\n\t" \
    "s_add_u32 s42, s42, 2048\n\t" \
    "buffer_load_dwordx4 a[24:27], v0, s[8:11], s42 offen\n\t" \
    "buffer_load_dwordx4 a[28:31], v0, s[8:11], s42 offen offset:1024\n\t" \
    "s_add_u32 s42, s42, 2048\n\t" \
    "buffer_load_dwordx4 a[32:35], v0, s[8:11], s42 offen\n\t" \
    "buffer_load_dwordx4 a[36:39], v0, s[8:11], s42 offen offset:1024\n\t" \
    "s_add_u32 s42, s42, 2048\n\t" \
    "buffer_load_dwordx4 a[40:43], v0, s[8:11], s42 offen\n\t" \
    "buffer_load_dwordx4 a[44:47], v0, s[8:11], s42 offen offset:1024\n\t" \
    "s_add_u32 s42, s42, 2048\n\t" \
    "buffer_load_dwordx4 a[48:51], v0, s[8:11], s42 offen\n\t" \
    "buffer_load_dwordx4 a[52:55], v0, s[8:11], s42 offen offset:1024\n\t" \
    "s_add_u32 s42, s42, 2048\n\t" \
    "buffer_load_dwordx4 a[56:59], v0, s[8:11], s42 offen\n\t" \
    "buffer_load_dwordx4 a[60:63], v0, s[8:11], s42 offen offset:1024\n\t" \
    "s_add_u32 s42, s42, 2048\n\t" \
    "buffer_load_dwordx4 a[64:67], v0, s[8:11], s42 offen\n\t" \
    "buffer_load_dwordx4 a[68:71], v0, s[8:11], s42 offen offset:1024\n\t" \
    "s_add_u32 s42, s42, 2048\n\t" \
    "buffer_load_dwordx4 a[72:75], v0, s[8:11], s42 offen\n\t" \
    "buffer_load_dwordx4 a[76:79], v0, s[8:11], s42 offen offset:1024\n\t" \
    "s_add_u32 s42, s42, 2048\n\t" \
    "buffer_load_dwordx4 a[80:83], v0, s[8:11], s42 offen\n\t" \
    "buffer_load_dwordx4 a[84:87], v0, s[8:11], s42 offen offset:1024\n\t" \
    "s_add_u32 s42, s42, 2048\n\t" \
    "buffer_load_dwordx4 a[88:91], v0, s[8:11], s42 offen\n\t" \
    "buffer_load_dwordx4 a[92:95], v0, s[8:11], s42 offen offset:1024\n\t" \
    "s_add_u32 s42, s42, 2048\n\t" \
    "buffer_load_dwordx4 a[96:99], v0, s[8:11], s42 offen\n\t" \
    "buffer_load_dwordx4 a[100:103], v0, s[8:11], s42 offen offset:1024\n\t" \
    "s_add_u32 s42, s42, 2048\n\t" \
    "buffer_load_dwordx4 a[104:107], v0, s[8:11], s42 offen\n\t" \
    "buffer_load_dwordx4 a[108:111], v0, s[8:11], s42 offen offset:1024\n\t" \
    "s_add_u32 s42, s42, 2048\n\t" \
    "buffer_load_dwordx4 a[112:115], v0, s[8:11], s42 offen\n\t" \
    "buffer_load_dwordx4 a[116:119], v0, s[8:11], s42 offen offset:1024\n\t" \
    "s_add_u32 s42, s42, 2048\n\t" \
    "buffer_load_dwordx4 a[120:123], v0, s[8:11], s42 offen\n\t" \
    "buffer_load_dwordx4 a[124:127], v0, s[8:11], s42 offen offset:1024\n\t" \
    "s_add_u32 s42, s42, 2048\n\t" \
    "buffer_load_dwordx4 a[128:131], v0, s[8:11], s42 offen\n\t" \
    "buffer_load_dwordx4 a[132:135], v0, s[8:11], s42 offen offset:1024\n\t" \
    "s_add_u32 s42, s42, 2048\n\t" \
    "buffer_load_dwordx4 a[136:139], v0, s[8:11], s42 offen\n\t" \
    "buffer_load_dwordx4 a[140:143], v0, s[8:11], s42 offen offset:1024\n\t" \
    "s_add_u32 s42, s42, 2048\n\t" \
    "buffer_load_dwordx4 a[144:147], v0, s[8:11], s42 offen\n\t" \
    "buffer_load_dwordx4 a[148:151], v0, s[8:11], s42 offen offset:1024\n\t" \
    "s_add_u32 s42, s42, 2048\n\t" \
    "buffer_load_dwordx4 a[152:155], v0, s[8:11], s42 offen\n\t" \
    "buffer_load_dwordx4 a[156:159], v0, s[8:11], s42 offen offset:1024\n\t" \
    "s_add_u32 s42, s42, 2048\n\t" \
    "buffer_load_dwordx4 a[160:163], v0, s[8:11], s42 offen\n\t" \
    "buffer_load_dwordx4 a[164:167], v0, s[8:11], s42 offen offset:1024\n\t" \
    "s_add_u32 s42, s42, 2048\n\t" \
    "buffer_load_dwordx4 a[168:171], v0, s[8:11], s42 offen\n\t" \
    "buffer_load_dwordx4 a[172:175], v0, s[8:11], s42 offen offset:1024\n\t" \
    "s_add_u32 s42, s42, 2048\n\t" \
    "buffer_load_dwordx4 a[176:179], v0, s[8:11], s42 offen\n\t" \
    "buffer_load_dwordx4 a[180:183], v0, s[8:11], s42 offen offset:1024\n\t" \
    "s_add_u32 s42, s42, 2048\n\t" \
    "buffer_load_dwordx4 a[184:187], v0, s[8:11], s42 offen\n\t" \
    "buffer_load_dwordx4 a[188:191], v0, s[8:11], s42 offen offset:1024\n\t" \
    "s_add_u32 s42, s42, 2048\n\t" \
    "buffer_load_dwordx4 a[192:195], v0, s[8:11], s42 offen\n\t" \
    "buffer_load_dwordx4 a[196:199], v0, s[8:11], s42 offen offset:1024\n\t" \
    "s_add_u32 s42, s42, 2048\n\t" \
    "buffer_load_dwordx4 a[200:203], v0, s[8:11], s42 offen\n\t" \
    "buffer_load_dwordx4 a[204:207], v0, s[8:11], s42 offen offset:1024\n\t" \
    "s_add_u32 s42, s42, 2048\n\t" \
    "buffer_load_dwordx4 a[208:211], v0, s[8:11], s42 offen\n\t" \
    "buffer_load_dwordx4 a[212:215], v0, s[8:11], s42 offen offset:1024\n\t" \
    "s_add_u32 s42, s42, 2048\n\t" \
    "s_waitcnt vmcnt(52)\n\t" \
    "buffer_load_dwordx4 a[216:219], v0, s[8:11], s42 offen\n\t" \
    "buffer_load_dwordx4 a[220:223], v0, s[8:11], s42 offen offset:1024\n\t" \
    "s_add_u32 s42, s42, 2048\n\t" \
    "s_waitcnt vmcnt(52)\n\t" \
    "buffer_load_dwordx4 a[224:227], v0, s[8:11], s42 offen\n\t" \
    "buffer_load_dwordx4 a[228:231], v0, s[8:11], s42 offen offset:1024\n\t" \
    "s_add_u32 s42, s42, 2048\n\t" \
    "s_waitcnt vmcnt(52)\n\t" \
    "buffer_load_dwordx4 a[232:235], v0, s[8:11], s42 offen\n\t" \
    "buffer_load_dwordx4 a[236:239], v0, s[8:11], s42 offen offset:1024\n\t" \
    "s_add_u32 s42, s42, 2048\n\t" \
    "s_waitcnt vmcnt(52)\n\t" \
    "buffer_load_dwordx4 a[240:243], v0, s[8:11], s42 offen\n\t" \
    "buffer_load_dwordx4 a[244:247], v0, s[8:11], s42 offen offset:1024\n\t" \
    "s_add_u32 s42, s42, 2048\n\t" \
    "s_waitcnt vmcnt(52)\n\t" \
    "buffer_load_dwordx4 a[248:251], v0, s[8:11], s42 offen\n\t" \
    "buffer_load_dwordx4 a[252:255], v0, s[8:11], s42 offen offset:1024\n\t" \
    "s_add_u32 s42, s42, 2048\n\t" \
    "s_waitcnt vmcnt(52)\n\t" \
    "buffer_load_dwordx4 v[120:123], v0, s[8:11], s42 offen\n\t" \
    "buffer_load_dwordx4 v[124:127], v0, s[8:11], s42 offen offset:1024\n\t" \
    "s_add_u32 s42, s42, 2048\n\t" \
    "s_waitcnt vmcnt(52)\n\t" \
    "buffer_load_dwordx4 v[128:131], v0, s[8:11], s42 offen\n\t" \
    "buffer_load_dwordx4 v[132:135], v0, s[8:11], s42 offen offset:1024\n\t" \
    "s_add_u32 s42, s42, 2048\n\t" \
    "s_waitcnt vmcnt(52)\n\t" \
    "buffer_load_dwordx4 v[136:139], v0, s[8:11], s42 offen\n\t" \
    "buffer_load_dwordx4 v[140:143], v0, s[8:11], s42 offen offset:1024\n\t" \
    "s_add_u32 s42, s42, 2048\n\t" \
    "s_waitcnt vmcnt(52)\n\t" \
    "buffer_load_dwordx4 v[144:147], v0, s[8:11], s42 offen\n\t" \
    "buffer_load_dwordx4 v[148:151], v0, s[8:11], s42 offen offset:1024\n\t" \
    "s_add_u32 s42, s42, 2048\n\t" \
    "s_waitcnt vmcnt(52)\n\t" \
    "buffer_load_dwordx4 v[152:155], v0, s[8:11], s42 offen\n\t" \
    "buffer_load_dwordx4 v[156:159], v0, s[8:11], s42 offen offset:1024\n\t" \
    "s_add_u32 s42, s42, 2048\n\t" \
    "s_waitcnt vmcnt(52)\n\t" \
    "buffer_load_dwordx4 v[160:163], v0, s[8:11], s42 offen\n\t" \
    "buffer_load_dwordx4 v[164:167], v0, s[8:11], s42 offen offset:1024\n\t" \
    "s_add_u32 s42, s42, 2048\n\t" \
    "s_waitcnt vmcnt(52)\n\t" \
    "buffer_load_dwordx4 v[168:171], v0, s[8:11], s42 offen\n\t" \
    "buffer_load_dwordx4 v[172:175], v0, s[8:11], s42 offen offset:1024\n\t" \
    "s_add_u32 s42, s42, 2048\n\t" \
    "s_waitcnt vmcnt(52)\n\t" \
    "buffer_load_dwordx4 v[176:179], v0, s[8:11], s42 offen\n\t" \
    "buffer_load_dwordx4 v[180:183], v0, s[8:11], s42 offen offset:1024\n\t" \
    "s_add_u32 s42, s42, 2048\n\t" \
    "s_waitcnt vmcnt(52)\n\t" \
    "buffer_load_dwordx4 v[184:187], v0, s[8:11], s42 offen\n\t" \
    "buffer_load_dwordx4 v[188:191], v0, s[8:11], s42 offen offset:1024\n\t" \
    "s_add_u32 s42, s42, 2048\n\t" \
    "s_waitcnt vmcnt(52)\n\t" \
    "buffer_load_dwordx4 v[192:195], v0, s[8:11], s42 offen\n\t" \
    "buffer_load_dwordx4 v[196:199], v0, s[8:11], s42 offen offset:1024\n\t" \
    "s_add_u32 s42, s42, 2048\n\t" \
    "s_waitcnt vmcnt(52)\n\t" \
    "buffer_load_dwordx4 v[200:203], v0, s[8:11], s42 offen\n\t" \
    "buffer_load_dwordx4 v[204:207], v0, s[8:11], s42 offen offset:1024\n\t" \
    "s_add_u32 s42, s42, 2048\n\t" \
    "s_waitcnt vmcnt(52)\n\t" \
    "buffer_load_dwordx4 v[208:211], v0, s[8:11], s42 offen\n\t" \
    "buffer_load_dwordx4 v[212:215], v0, s[8:11], s42 offen offset:1024\n\t" \
    "s_add_u32 s42, s42, 2048\n\t" \
    "s_waitcnt vmcnt(52)\n\t" \
    "buffer_load_dwordx4 v[216:219], v0, s[8:11], s42 offen\n\t" \
    "buffer_load_dwordx4 v[220:223], v0, s[8:11], s42 offen offset:1024\n\t" \
    "s_add_u32 s42, s42, 2048\n\t" \
    "s_waitcnt vmcnt(52)\n\t" \
    "buffer_load_dwordx4 v[224:227], v0, s[8:11], s42 offen\n\t" \
    "buffer_load_dwordx4 v[228:231], v0, s[8:11], s42 offen offset:1024\n\t" \
    "s_add_u32 s42, s42, 2048\n\t" \
    "s_waitcnt vmcnt(52)\n\t" \
    "buffer_load_dwordx4 v[232:235], v0, s[8:11], s42 offen\n\t" \
    "buffer_load_dwordx4 v[236:239], v0, s[8:11], s42 offen offset:1024\n\t" \
    "s_add_u32 s42, s42, 2048\n\t" \
    "s_waitcnt vmcnt(52)\n\t" \
    "buffer_load_dwordx4 v[240:243], v0, s[8:11], s42 offen\n\t" \
    "buffer_load_dwordx4 v[244:247], v0, s[8:11], s42 offen offset:1024\n\t" \
    "s_add_u32 s42, s42, 2048\n\t" \
    "s_waitcnt vmcnt(52)\n\t" \
    "buffer_load_dwordx4 v[248:251], v0, s[8:11], s42 offen\n\t" \
    "buffer_load_dwordx4 v[252:255], v0, s[8:11], s42 offen offset:1024\n\t" \
    "s_add_u32 s42, s42, 2048\n\t" \
    "s_waitcnt vmcnt(52)\n\t" \
    "s_mov_b32 s43, s14\n\t" \
    "s_add_u32 s44, s43, 4096\n\t" \
    "buffer_load_dwordx4 v[72:75], v0, s[8:11], s43 offen\n\t" \
    "buffer_load_dwordx4 v[76:79], v0, s[8:11], s43 offen offset:1024\n\t" \
    "buffer_load_dwordx4 v[80:83], v0, s[8:11], s43 offen offset:2048\n\t" \
    "buffer_load_dwordx4 v[84:87], v0, s[8:11], s43 offen offset:3072\n\t" \
    "buffer_load_dwordx4 v[88:91], v0, s[8:11], s44 offen\n\t" \
    "buffer_load_dwordx4 v[92:95], v0, s[8:11], s44 offen offset:1024\n\t" \
    "buffer_load_dwordx4 v[96:99], v0, s[8:11], s44 offen offset:2048\n\t" \
    "buffer_load_dwordx4 v[100:103], v0, s[8:11], s44 offen offset:3072\n\t" \
    "s_add_u32 s43, s43, 32768\n\t" \
    "s_mov_b32 s28, 0\n\t" \
    "s_mov_b32 s29, 0\n\t" \
    "s_mov_b32 s30, 0\n\t" \
    "s_mov_b32 s46, 0xaaaaaaaa\n\t" \
    "s_mov_b32 s47, 0xaaaaaaaa\n\t" \
    "s_mov_b32 s48, -1\n\t" \
    "s_mov_b32 s49, 0\n\t" \
    "s_mov_b32 s50, -1\n\t" \
    "s_mov_b32 s51, 0x00550055\n\t" \
    "s_memrealtime s[58:59]\n\t" \
    "s_waitcnt vmcnt(0) lgkmcnt(0)\n\t" \
    "s_mov_b32 s59, 0\n\t" \
    "LA_loop_%=:\n\t" \
    "s_cmp_eq_u32 s28, 0\n\t" \
    "s_cbranch_scc1 LA_nosched_%=\n\t" \
    "s_mov_b32 s61, 0\n\t" \
    "s_waitcnt vmcnt(9)\n\t" \
    "LA_schk_%=:\n\t" \
    "v_cmp_eq_u32 vcc, -1, v19\n\t" \
    "s_add_u32 s61, s61, 1\n\t" \
    "s_cmp_gt_u32 s61, 131072\n\t" \
    "s_cbranch_scc1 LA_sok_%=\n\t" \
    "s_cbranch_vccz LA_sok_%=\n\t" \
    "buffer_load_dword v19, v23, s[8:11], s62 offen sc1\n\t" \
    "s_waitcnt vmcnt(0)\n\t" \
    "s_branch LA_schk_%=\n\t" \
    "LA_sok_%=:\n\t" \
    "v_subrev_u32 v19, s58, v19\n\t" \
    "s_nop 1\n\t" \
    "v_max_i32_dpp v19, v19, v19 row_ror:8 row_mask:0xf bank_mask:0xf\n\t" \
    "s_nop 1\n\t" \
    "v_max_i32_dpp v19, v19, v19 row_ror:4 row_mask:0xf bank_mask:0xf\n\t" \
    "s_nop 1\n\t" \
    "v_max_i32_dpp v19, v19, v19 row_ror:2 row_mask:0xf bank_mask:0xf\n\t" \
    "s_nop 1\n\t" \
    "v_max_i32_dpp v19, v19, v19 row_ror:1 row_mask:0xf bank_mask:0xf\n\t" \
    "s_nop 1\n\t" \
    "v_readlane_b32 s42, v19, 0\n\t" \
    "v_readlane_b32 s45, v19, 16\n\t" \
    "s_max_i32 s42, s42, s45\n\t" \
    "v_readlane_b32 s45, v19, 32\n\t" \
    "v_readlane_b32 s63, v19, 48\n\t" \
    "s_max_i32 s45, s45, s63\n\t" \
    "s_max_i32 s42, s42, s45\n\t" \
    "s_add_u32 s58, s58, s42\n\t" \
    "s_add_u32 s63, s58, s52\n\t" \
    "LA_nosched_%=:\n\t" \
    "s_lshr_b32 s42, s30, 5\n\t" \
    "s_lshl_b32 s42, s42, 2\n\t" \
    "s_and_b32 s45, s30, 1\n\t" \
    "s_lshl_b32 s45, s45, 1\n\t" \
    "s_add_u32 s42, s42, s45\n\t" \
    "s_lshr_b32 s42, s12, s42\n\t" \
    "s_and_b32 s42, s42, 3\n\t" \
    "s_lshl_b32 s42, s42, 13\n\t" \
    "s_lshr_b32 s45, s30, 1\n\t" \
    "s_lshl_b32 s45, s45, 15\n\t" \
    "s_add_u32 s31, s42, s45\n\t" \
    "s_add_u32 s33, s31, s16\n\t" \
    "s_add_u32 s34, s31, s17\n\t" \
    "s_add_u32 s30, s30, 1\n\t" \
    "s_lshr_b32 s42, s30, 5\n\t" \
    "s_lshl_b32 s42, s42, 2\n\t" \
    "s_and_b32 s45, s30, 1\n\t" \
    "s_lshl_b32 s45, s45, 1\n\t" \
    "s_add_u32 s42, s42, s45\n\t" \
    "s_lshr_b32 s42, s12, s42\n\t" \
    "s_and_b32 s42, s42, 3\n\t" \
    "s_lshl_b32 s42, s42, 13\n\t" \
    "s_lshr_b32 s45, s30, 1\n\t" \
    "s_lshl_b32 s45, s45, 15\n\t" \
    "s_add_u32 s31, s42, s45\n\t" \
    "s_add_u32 s30, s30, 1\n\t" \
    "s_lshr_b32 s42, s30, 5\n\t" \
    "s_lshl_b32 s42, s42, 2\n\t" \
    "s_and_b32 s45, s30, 1\n\t" \
    "s_lshl_b32 s45, s45, 1\n\t" \
    "s_add_u32 s42, s42, s45\n\t" \
    "s_lshr_b32 s42, s12, s42\n\t" \
    "s_and_b32 s42, s42, 3\n\t" \
    "s_lshl_b32 s42, s42, 13\n\t" \
    "s_lshr_b32 s45, s30, 1\n\t" \
    "s_lshl_b32 s45, s45, 15\n\t" \
    "s_add_u32 s32, s42, s45\n\t" \
    "s_add_u32 s30, s30, 1\n\t" \
    "s_add_u32 s62, s31, 5120\n\t" \
    "s_add_u32 s35, s31, s24\n\t" \
    "v_mov_b32 v11, s31\n\t" \
    "v_mov_b32 v10, s32\n\t" \
    "v_cndmask_b32 v11, v11, v10, s[26:27]\n\t" \
    "v_add_u32 v11, v11, v8\n\t" \
    "v_add_u32 v25, s15, v0\n\t" \
    "v_add_u32 v29, s55, v0\n\t" \
    "v_add_u32 v24, s18, v0\n\t" \
    "v_add_u32 v28, s19, v0\n\t" \
    "v_add_u32 v32, s20, v0\n\t" \
    "v_add_u32 v36, s21, v0\n\t" \
    "v_add_u32 v40, s22, v0\n\t" \
    "s_cmp_eq_u32 s28, 0\n\t" \
    "s_cbranch_scc1 LA_retry_%=\n\t" \
    "LA_aw_%=:\n\t" \
    "s_memrealtime s[56:57]\n\t" \
    "s_waitcnt lgkmcnt(0)\n\t" \
    "s_sub_u32 s42, s63, s56\n\t" \
    "s_cmp_gt_i32 s42, 400\n\t" \
    "s_cselect_b32 s42, 0, s42\n\t" \
    "s_cmp_gt_i32 s42, 6\n\t" \
    "s_cbranch_scc1 LA_aw_%=\n\t" \
    "s_mul_i32 s42, s42, 3\n\t" \
    "s_sub_u32 s42, s42, 8\n\t" \
    "s_max_i32 s42, s42, 0\n\t" \
    "s_min_i32 s42, s42, 16\n\t" \
    "s_lshl_b32 s42, s42, 2\n\t" \
    "s_sub_u32 s42, 76, s42\n\t" \
    "s_getpc_b64 s[40:41]\n\t" \
    "s_add_u32 s40, s40, s42\n\t" \
    "s_addc_u32 s41, s41, 0\n\t" \
    "s_setpc_b64 s[40:41]\n\t" \
    ".rept 16\n\t" \
    "s_nop 1\n\t" \
    ".endr\n\t" \
    "LA_retry_%=:\n\t" \
    "buffer_load_dwordx4 v[16:19], v0, s[8:11], s33 offen sc1\n\t" \
    "buffer_load_dwordx4 v[20:23], v0, s[8:11], s34 offen sc1\n\t" \
    "v_mov_b64 v[44:45], 0\n\t" \
    "v_mov_b64 v[46:47], 0\n\t" \
    "v_mov_b64 v[48:49], 0\n\t" \
    "v_mov_b64 v[50:51], 0\n\t" \
    "v_mov_b64 v[52:53], 0\n\t" \
    "v_mov_b64 v[54:55], 0\n\t" \
    "v_mov_b64 v[56:57], 0\n\t" \
    "v_mov_b64 v[58:59], 0\n\t" \
    "v_mov_b64 v[60:61], 0\n\t" \
    "v_mov_b64 v[62:63], 0\n\t" \
    "v_mov_b64 v[64:65], 0\n\t" \
    "v_mov_b64 v[66:67], 0\n\t" \
    "v_mov_b64 v[68:69], 0\n\t" \
    "v_mov_b64 v[70:71], 0\n\t" \
    "v_mov_b64 v[108:109], 0\n\t" \
    "v_mov_b64 v[110:111], 0\n\t" \
    "v_mov_b64 v[112:113], 0\n\t" \
    "v_mov_b64 v[114:115], 0\n\t" \
    "v_mov_b32 v104, v9\n\t" \
    "v_mov_b32 v105, v9\n\t" \
    "v_mov_b32 v106, v9\n\t" \
    "v_mov_b32 v107, v9\n\t" \
    "s_waitcnt vmcnt(1)\n\t" \
    "s_memrealtime s[56:57]\n\t" \
    "ds_write_b128 v25, v[16:19]\n\t" \
    "v_smfmac_f32_16x16x64_bf16 v[44:47], v[16:19], a[0:7], v1\n\t" \
    "v_max3_u32 v10, v16, v17, v18\n\t" \
    "v_smfmac_f32_16x16x64_bf16 v[48:51], v[16:19], a[8:15], v1\n\t" \
    "v_max_u32 v10, v10, v19\n\t" \
    "v_smfmac_f32_16x16x64_bf16 v[52:55], v[16:19], a[16:23], v1\n\t" \
    "v_smfmac_f32_16x16x64_bf16 v[56:59], v[16:19], a[24:31], v1\n\t" \
    "s_waitcnt vmcnt(0)\n\t" \
    "ds_write_b128 v29, v[20:23]\n\t" \
    "v_smfmac_f32_16x16x64_bf16 v[60:63], v[16:19], a[32:39], v1\n\t" \
    "v_max3_u32 v10, v20, v21, v10\n\t" \
    "v_smfmac_f32_16x16x64_bf16 v[64:67], v[16:19], a[40:47], v1\n\t" \
    "v_max3_u32 v10, v22, v23, v10\n\t" \
    "v_cmp_eq_u32 vcc, -1, v10\n\t" \
    "v_smfmac_f32_16x16x64_bf16 v[68:71], v[16:19], a[48:55], v1\n\t" \
    "s_cbranch_vccnz LA_fail_%=\n\t" \
    "v_smfmac_f32_16x16x64_bf16 v[44:47], v[20:23], a[56:63], v1\n\t" \
    "s_waitcnt lgkmcnt(0)\n\t" \
    "s_barrier\n\t" \
    "ds_read_b128 v[24:27], v24\n\t" \
    "ds_read_b128 v[28:31], v28\n\t" \
    "ds_read_b128 v[32:35], v32\n\t" \
    "ds_read_b128 v[36:39], v36\n\t" \
    "ds_read_b128 v[40:43], v40\n\t" \
    "v_mov_b32 v10, s56\n\t" \
    "v_min_u32 v10, -2, v10\n\t" \
    "s_mov_b64 exec, 1\n\t" \
    "buffer_store_dword v10, off, s[8:11], s35 sc1\n\t" \
    "s_mov_b64 exec, -1\n\t" \
    "v_smfmac_f32_16x16x64_bf16 v[48:51], v[20:23], a[64:71], v1\n\t" \
    "v_smfmac_f32_16x16x64_bf16 v[52:55], v[20:23], a[72:79], v1\n\t" \
    "v_smfmac_f32_16x16x64_bf16 v[56:59], v[20:23], a[80:87], v1\n\t" \
    "v_smfmac_f32_16x16x64_bf16 v[60:63], v[20:23], a[88:95], v1\n\t" \
    "v_smfmac_f32_16x16x64_bf16 v[64:67], v[20:23], a[96:103], v1\n\t" \
    "v_smfmac_f32_16x16x64_bf16 v[68:71], v[20:23], a[104:111], v1\n\t" \
    "s_waitcnt lgkmcnt(4)\n\t" \
    "v_smfmac_f32_16x16x64_bf16 v[44:47], v[24:27], a[112:119], v1\n\t" \
    "v_smfmac_f32_16x16x64_bf16 v[48:51], v[24:27], a[120:127], v1\n\t" \
    "v_smfmac_f32_16x16x64_bf16 v[52:55], v[24:27], a[128:135], v1\n\t" \
    "v_smfmac_f32_16x16x64_bf16 v[56:59], v[24:27], a[136:143], v1\n\t" \
    "v_smfmac_f32_16x16x64_bf16 v[60:63], v[24:27], a[144:151], v1\n\t" \
    "v_smfmac_f32_16x16x64_bf16 v[64:67], v[24:27], a[152:159], v1\n\t" \
    "v_smfmac_f32_16x16x64_bf16 v[68:71], v[24:27], a[160:167], v1\n\t" \
    "s_waitcnt lgkmcnt(3)\n\t" \
    "v_smfmac_f32_16x16x64_bf16 v[44:47], v[28:31], a[168:175], v1\n\t" \
    "v_smfmac_f32_16x16x64_bf16 v[48:51], v[28:31], a[176:183], v1\n\t" \
    "v_smfmac_f32_16x16x64_bf16 v[52:55], v[28:31], a[184:191], v1\n\t" \
    "v_smfmac_f32_16x16x64_bf16 v[56:59], v[28:31], a[192:199], v1\n\t" \
    "v_smfmac_f32_16x16x64_bf16 v[60:63], v[28:31], a[200:207], v1\n\t" \
    "v_smfmac_f32_16x16x64_bf16 v[64:67], v[28:31], a[208:215], v1\n\t" \
    "v_smfmac_f32_16x16x64_bf16 v[68:71], v[28:31], a[216:223], v1\n\t" \
    "s_waitcnt lgkmcnt(2)\n\t" \
    "v_smfmac_f32_16x16x64_bf16 v[44:47], v[32:35], a[224:231], v1\n\t" \
    "v_smfmac_f32_16x16x64_bf16 v[48:51], v[32:35], a[232:239], v1\n\t" \
    "v_smfmac_f32_16x16x64_bf16 v[52:55], v[32:35], a[240:247], v1\n\t" \
    "v_smfmac_f32_16x16x64_bf16 v[56:59], v[32:35], a[248:255], v1\n\t" \
    "v_smfmac_f32_16x16x64_bf16 v[60:63], v[32:35], v[120:127], v1\n\t" \
    "v_smfmac_f32_16x16x64_bf16 v[64:67], v[32:35], v[128:135], v1\n\t" \
    "v_smfmac_f32_16x16x64_bf16 v[68:71], v[32:35], v[136:143], v1\n\t" \
    "s_waitcnt lgkmcnt(1)\n\t" \
    "v_smfmac_f32_16x16x64_bf16 v[44:47], v[36:39], v[144:151], v1\n\t" \
    "ds_read2_b64 v[16:19], v2 offset0:0 offset1:1\n\t" \
    "v_smfmac_f32_16x16x64_bf16 v[48:51], v[36:39], v[152:159], v1\n\t" \
    "ds_read2_b64 v[20:23], v3 offset0:0 offset1:1\n\t" \
    "v_smfmac_f32_16x16x64_bf16 v[52:55], v[36:39], v[160:167], v1\n\t" \
    "ds_read2_b64 v[24:27], v4 offset0:0 offset1:1\n\t" \
    "v_smfmac_f32_16x16x64_bf16 v[56:59], v[36:39], v[168:175], v1\n\t" \
    "ds_read2_b64 v[28:31], v5 offset0:0 offset1:1\n\t" \
    "v_smfmac_f32_16x16x64_bf16 v[60:63], v[36:39], v[176:183], v1\n\t" \
    "v_smfmac_f32_16x16x64_bf16 v[64:67], v[36:39], v[184:191], v1\n\t" \
    "v_smfmac_f32_16x16x64_bf16 v[68:71], v[36:39], v[192:199], v1\n\t" \
    "s_waitcnt lgkmcnt(4)\n\t" \
    "v_smfmac_f32_16x16x64_bf16 v[44:47], v[40:43], v[200:207], v1\n\t" \
    "v_smfmac_f32_16x16x64_bf16 v[48:51], v[40:43], v[208:215], v1\n\t" \
    "v_smfmac_f32_16x16x64_bf16 v[52:55], v[40:43], v[216:223], v1\n\t" \
    "v_smfmac_f32_16x16x64_bf16 v[56:59], v[40:43], v[224:231], v1\n\t" \
    "v_smfmac_f32_16x16x64_bf16 v[60:63], v[40:43], v[232:239], v1\n\t" \
    "v_cvt_pk_bf16_f32 v44, v44, v45\n\t" \
    "v_cvt_pk_bf16_f32 v45, v46, v47\n\t" \
    "v_cvt_pk_bf16_f32 v46, v48, v49\n\t" \
    "v_smfmac_f32_16x16x64_bf16 v[64:67], v[40:43], v[240:247], v1\n\t" \
    "v_cvt_pk_bf16_f32 v47, v50, v51\n\t" \
    "v_cvt_pk_bf16_f32 v52, v52, v53\n\t" \
    "s_nop 0\n\t" \
    "v_smfmac_f32_16x16x64_bf16 v[68:71], v[40:43], v[248:255], v1\n\t" \
    "v_cvt_pk_bf16_f32 v53, v54, v55\n\t" \
    "v_cvt_pk_bf16_f32 v54, v56, v57\n\t" \
    "v_cvt_pk_bf16_f32 v55, v58, v59\n\t" \
    "v_mfma_f32_16x16x32_bf16 v[48:51], v[44:47], v[12:15], 0\n\t" \
    "v_cvt_pk_bf16_f32 v60, v60, v61\n\t" \
    "v_cvt_pk_bf16_f32 v61, v62, v63\n\t" \
    "v_cvt_pk_bf16_f32 v62, v64, v65\n\t" \
    "v_mfma_f32_16x16x32_bf16 v[56:59], v[52:55], v[12:15], 0\n\t" \
    "v_cvt_pk_bf16_f32 v63, v66, v67\n\t" \
    "v_cvt_pk_bf16_f32 v116, v68, v69\n\t" \
    "v_cvt_pk_bf16_f32 v117, v70, v71\n\t" \
    "v_mfma_f32_16x16x32_bf16 v[64:67], v[60:63], v[12:15], 0\n\t" \
    "v_cvt_pk_bf16_f32 v118, v68, v69\n\t" \
    "v_cvt_pk_bf16_f32 v119, v70, v71\n\t" \
    "v_cvt_pk_bf16_f32 v44, v48, v49\n\t" \
    "v_cvt_pk_bf16_f32 v45, v50, v51\n\t" \
    "v_mfma_f32_16x16x32_bf16 v[68:71], v[116:119], v[12:15], 0\n\t" \
    "v_cvt_pk_bf16_f32 v46, v56, v57\n\t" \
    "v_cvt_pk_bf16_f32 v47, v58, v59\n\t" \
    "v_mov_b32 v55, 0\n\t" \
    "v_cvt_pk_bf16_f32 v52, v64, v65\n\t" \
    "v_smfmac_f32_16x16x64_bf16 v[108:111], v[44:47], v[72:79], v1\n\t" \
    "v_cvt_pk_bf16_f32 v53, v66, v67\n\t" \
    "s_nop 1\n\t" \
    "v_smfmac_f32_16x16x64_bf16 v[112:115], v[44:47], v[88:95], v1\n\t" \
    "v_cvt_pk_bf16_f32 v116, v68, v69\n\t" \
    "v_cvt_pk_bf16_f32 v117, v70, v71\n\t" \
    "s_waitcnt lgkmcnt(2)\n\t" \
    "v_cndmask_b32 v54, v116, v117, s[46:47]\n\t" \
    "v_smfmac_f32_16x16x64_bf16 v[104:107], v[44:47], v[16:23], v1\n\t" \
    "s_waitcnt lgkmcnt(0)\n\t" \
    "v_smfmac_f32_16x16x64_bf16 v[108:111], v[52:55], v[80:87], v1\n\t" \
    "v_smfmac_f32_16x16x64_bf16 v[112:115], v[52:55], v[96:103], v1\n\t" \
    "v_smfmac_f32_16x16x64_bf16 v[104:107], v[52:55], v[24:31], v1\n\t" \
    "s_nop 5\n\t" \
    "v_add_f32 v16, v108, v109\n\t" \
    "v_add_f32 v20, v110, v111\n\t" \
    "v_add_f32 v17, v112, v113\n\t" \
    "v_add_f32 v21, v114, v115\n\t" \
    "v_add_f32 v18, v104, v105\n\t" \
    "v_add_f32 v22, v106, v107\n\t" \
    "ds_write_b96 v6, v[16:18]\n\t" \
    "ds_write_b96 v6, v[20:22] offset:256\n\t" \
    "v_lshlrev_b32 v23, 1, v0\n\t" \
    "v_and_b32 v23, 0x780, v23\n\t" \
    "v_bfe_u32 v10, v0, 4, 2\n\t" \
    "v_lshl_or_b32 v23, v10, 2, v23\n\t" \
    "buffer_load_dword v19, v23, s[8:11], s62 offen sc1\n\t" \
    "s_waitcnt lgkmcnt(0)\n\t" \
    "s_barrier\n\t" \
    "ds_read_b96 v[16:18], v7\n\t" \
    "ds_read_b96 v[20:22], v7 offset:2048\n\t" \
    "ds_read_b96 v[24:26], v7 offset:4096\n\t" \
    "ds_read_b96 v[28:30], v7 offset:6144\n\t" \
    "s_waitcnt lgkmcnt(2)\n\t" \
    "v_pk_add_f32 v[16:17], v[16:17], v[20:21]\n\t" \
    "v_add_f32 v18, v18, v22\n\t" \
    "s_waitcnt lgkmcnt(0)\n\t" \
    "v_pk_add_f32 v[24:25], v[24:25], v[28:29]\n\t" \
    "v_add_f32 v26, v26, v30\n\t" \
    "v_pk_add_f32 v[16:17], v[16:17], v[24:25]\n\t" \
    "v_add_f32 v18, v18, v26\n\t" \
    "v_max3_f32 v20, v16, v17, v18\n\t" \
    "s_nop 1\n\t" \
    "v_max_f32_dpp v20, v20, v20 row_ror:8 row_mask:0xf bank_mask:0xf\n\t" \
    "s_nop 1\n\t" \
    "v_max_f32_dpp v20, v20, v20 row_ror:4 row_mask:0xf bank_mask:0xf\n\t" \
    "s_nop 1\n\t" \
    "v_max_f32_dpp v20, v20, v20 row_ror:2 row_mask:0xf bank_mask:0xf\n\t" \
    "s_nop 1\n\t" \
    "v_max_f32_dpp v20, v20, v20 row_ror:1 row_mask:0xf bank_mask:0xf\n\t" \
    "v_sub_f32 v16, v16, v20\n\t" \
    "v_sub_f32 v17, v17, v20\n\t" \
    "v_sub_f32 v18, v18, v20\n\t" \
    "v_exp_f32 v16, v16\n\t" \
    "v_exp_f32 v17, v17\n\t" \
    "v_exp_f32 v18, v18\n\t" \
    "v_add_f32 v21, v16, v17\n\t" \
    "v_add_f32 v21, v21, v18\n\t" \
    "v_mov_b32_dpp v22, v18 quad_perm:[1,1,3,3] row_mask:0xf bank_mask:0xf\n\t" \
    "v_cndmask_b32 v24, v18, v16, s[48:49]\n\t" \
    "v_add_f32_dpp v21, v21, v21 row_ror:8 row_mask:0xf bank_mask:0xf\n\t" \
    "v_cndmask_b32 v25, v22, v17, s[48:49]\n\t" \
    "s_nop 0\n\t" \
    "v_add_f32_dpp v21, v21, v21 row_ror:4 row_mask:0xf bank_mask:0xf\n\t" \
    "s_nop 1\n\t" \
    "v_add_f32_dpp v21, v21, v21 row_ror:2 row_mask:0xf bank_mask:0xf\n\t" \
    "s_nop 1\n\t" \
    "v_add_f32_dpp v21, v21, v21 row_ror:1 row_mask:0xf bank_mask:0xf\n\t" \
    "v_rcp_f32 v21, v21\n\t" \
    "s_nop 0\n\t" \
    "v_mul_f32 v24, v24, v21\n\t" \
    "v_mul_f32 v25, v25, v21\n\t" \
    "v_cvt_pk_bf16_f32 v24, v24, v25\n\t" \
    "v_min_u32 v24, -2, v24\n\t" \
    "s_mov_b64 exec, s[50:51]\n\t" \
    "buffer_store_dword v24, v11, s[8:11], 0 offen sc1\n\t" \
    "s_mov_b64 exec, -1\n\t" \
    "s_add_u32 s28, s28, 1\n\t" \
    "s_cmp_eq_u32 s28, 63\n\t" \
    "s_cbranch_scc1 LA_last_%=\n\t" \
    "s_add_u32 s44, s43, 4096\n\t" \
    "buffer_load_dwordx4 v[72:75], v0, s[8:11], s43 offen\n\t" \
    "buffer_load_dwordx4 v[76:79], v0, s[8:11], s43 offen offset:1024\n\t" \
    "buffer_load_dwordx4 v[80:83], v0, s[8:11], s43 offen offset:2048\n\t" \
    "buffer_load_dwordx4 v[84:87], v0, s[8:11], s43 offen offset:3072\n\t" \
    "buffer_load_dwordx4 v[88:91], v0, s[8:11], s44 offen\n\t" \
    "buffer_load_dwordx4 v[92:95], v0, s[8:11], s44 offen offset:1024\n\t" \
    "buffer_load_dwordx4 v[96:99], v0, s[8:11], s44 offen offset:2048\n\t" \
    "buffer_load_dwordx4 v[100:103], v0, s[8:11], s44 offen offset:3072\n\t" \
    "s_add_u32 s43, s43, 32768\n\t" \
    "s_mov_b32 s29, 0\n\t" \
    "s_branch LA_loop_%=\n\t" \
    "LA_fail_%=:\n\t" \
    "s_add_u32 s29, s29, 1\n\t" \
    "s_cmp_gt_u32 s29, 131072\n\t" \
    "s_cbranch_scc0 LA_retry_%=\n\t" \
    "LA_last_%=:\n\t" \
    "s_endpgm\n\t"
#define ASM_CHAIN_PV \
    "s_nop 7\n\t" \
    "s_mov_b32 s42, s13\n\t" \
    "buffer_load_dwordx4 a[0:3], v0, s[8:11], s42 offen\n\t" \
    "buffer_load_dwordx4 a[4:7], v0, s[8:11], s42 offen offset:1024\n\t" \
    "s_add_u32 s42, s42, 2048\n\t" \
    "buffer_load_dwordx4 a[8:11], v0, s[8:11], s42 offen\n\t" \
    "buffer_load_dwordx4 a[12:15], v0, s[8:11], s42 offen offset:1024\n\t" \
    "s_add_u32 s42, s42, 2048\n\t" \
    "buffer_load_dwordx4 a[16:19], v0, s[8:11], s42 offen\n\t" \
    "buffer_load_dwordx4 a[20:23], v0, s[8:11], s42 offen offset:1024\n\t" \
    "s_add_u32 s42, s42, 2048\n\t" \
    "buffer_load_dwordx4 a[24:27], v0, s[8:11], s42 offen\n\t" \
    "buffer_load_dwordx4 a[28:31], v0, s[8:11], s42 offen offset:1024\n\t" \
    "s_add_u32 s42, s42, 2048\n\t" \
    "buffer_load_dwordx4 a[32:35], v0, s[8:11], s42 offen\n\t" \
    "buffer_load_dwordx4 a[36:39], v0, s[8:11], s42 offen offset:1024\n\t" \
    "s_add_u32 s42, s42, 2048\n\t" \
    "buffer_load_dwordx4 a[40:43], v0, s[8:11], s42 offen\n\t" \
    "buffer_load_dwordx4 a[44:47], v0, s[8:11], s42 offen offset:1024\n\t" \
    "s_add_u32 s42, s42, 2048\n\t" \
    "buffer_load_dwordx4 a[48:51], v0, s[8:11], s42 offen\n\t" \
    "buffer_load_dwordx4 a[52:55], v0, s[8:11], s42 offen offset:1024\n\t" \
    "s_add_u32 s42, s42, 2048\n\t" \
    "buffer_load_dwordx4 a[56:59], v0, s[8:11], s42 offen\n\t" \
    "buffer_load_dwordx4 a[60:63], v0, s[8:11], s42 offen offset:1024\n\t" \
    "s_add_u32 s42, s42, 2048\n\t" \
    "buffer_load_dwordx4 a[64:67], v0, s[8:11], s42 offen\n\t" \
    "buffer_load_dwordx4 a[68:71], v0, s[8:11], s42 offen offset:1024\n\t" \
    "s_add_u32 s42, s42, 2048\n\t" \
    "buffer_load_dwordx4 a[72:75], v0, s[8:11], s42 offen\n\t" \
    "buffer_load_dwordx4 a[76:79], v0, s[8:11], s42 offen offset:1024\n\t" \
    "s_add_u32 s42, s42, 2048\n\t" \
    "buffer_load_dwordx4 a[80:83], v0, s[8:11], s42 offen\n\t" \
    "buffer_load_dwordx4 a[84:87], v0, s[8:11], s42 offen offset:1024\n\t" \
    "s_add_u32 s42, s42, 2048\n\t" \
    "buffer_load_dwordx4 a[88:91], v0, s[8:11], s42 offen\n\t" \
    "buffer_load_dwordx4 a[92:95], v0, s[8:11], s42 offen offset:1024\n\t" \
    "s_add_u32 s42, s42, 2048\n\t" \
    "buffer_load_dwordx4 a[96:99], v0, s[8:11], s42 offen\n\t" \
    "buffer_load_dwordx4 a[100:103], v0, s[8:11], s42 offen offset:1024\n\t" \
    "s_add_u32 s42, s42, 2048\n\t" \
    "buffer_load_dwordx4 a[104:107], v0, s[8:11], s42 offen\n\t" \
    "buffer_load_dwordx4 a[108:111], v0, s[8:11], s42 offen offset:1024\n\t" \
    "s_add_u32 s42, s42, 2048\n\t" \
    "buffer_load_dwordx4 a[112:115], v0, s[8:11], s42 offen\n\t" \
    "buffer_load_dwordx4 a[116:119], v0, s[8:11], s42 offen offset:1024\n\t" \
    "s_add_u32 s42, s42, 2048\n\t" \
    "buffer_load_dwordx4 a[120:123], v0, s[8:11], s42 offen\n\t" \
    "buffer_load_dwordx4 a[124:127], v0, s[8:11], s42 offen offset:1024\n\t" \
    "s_add_u32 s42, s42, 2048\n\t" \
    "buffer_load_dwordx4 a[128:131], v0, s[8:11], s42 offen\n\t" \
    "buffer_load_dwordx4 a[132:135], v0, s[8:11], s42 offen offset:1024\n\t" \
    "s_add_u32 s42, s42, 2048\n\t" \
    "buffer_load_dwordx4 a[136:139], v0, s[8:11], s42 offen\n\t" \
    "buffer_load_dwordx4 a[140:143], v0, s[8:11], s42 offen offset:1024\n\t" \
    "s_add_u32 s42, s42, 2048\n\t" \
    "buffer_load_dwordx4 a[144:147], v0, s[8:11], s42 offen\n\t" \
    "buffer_load_dwordx4 a[148:151], v0, s[8:11], s42 offen offset:1024\n\t" \
    "s_add_u32 s42, s42, 2048\n\t" \
    "buffer_load_dwordx4 a[152:155], v0, s[8:11], s42 offen\n\t" \
    "buffer_load_dwordx4 a[156:159], v0, s[8:11], s42 offen offset:1024\n\t" \
    "s_add_u32 s42, s42, 2048\n\t" \
    "buffer_load_dwordx4 a[160:163], v0, s[8:11], s42 offen\n\t" \
    "buffer_load_dwordx4 a[164:167], v0, s[8:11], s42 offen offset:1024\n\t" \
    "s_add_u32 s42, s42, 2048\n\t" \
    "buffer_load_dwordx4 a[168:171], v0, s[8:11], s42 offen\n\t" \
    "buffer_load_dwordx4 a[172:175], v0, s[8:11], s42 offen offset:1024\n\t" \
    "s_add_u32 s42, s42, 2048\n\t" \
    "buffer_load_dwordx4 a[176:179], v0, s[8:11], s42 offen\n\t" \
    "buffer_load_dwordx4 a[180:183], v0, s[8:11], s42 offen offset:1024\n\t" \
    "s_add_u32 s42, s42, 2048\n\t" \
    "buffer_load_dwordx4 a[184:187], v0, s[8:11], s42 offen\n\t" \
    "buffer_load_dwordx4 a[188:191], v0, s[8:11], s42 offen offset:1024\n\t" \
    "s_add_u32 s42, s42, 2048\n\t" \
    "buffer_load_dwordx4 a[192:195], v0, s[8:11], s42 offen\n\t" \
    "buffer_load_dwordx4 a[196:199], v0, s[8:11], s42 offen offset:1024\n\t" \
    "s_add_u32 s42, s42, 2048\n\t" \
    "buffer_load_dwordx4 a[200:203], v0, s[8:11], s42 offen\n\t" \
    "buffer_load_dwordx4 a[204:207], v0, s[8:11], s42 offen offset:1024\n\t" \
    "s_add_u32 s42, s42, 2048\n\t" \
    "buffer_load_dwordx4 a[208:211], v0, s[8:11], s42 offen\n\t" \
    "buffer_load_dwordx4 a[212:215], v0, s[8:11], s42 offen offset:1024\n\t" \
    "s_add_u32 s42, s42, 2048\n\t" \
    "s_waitcnt vmcnt(52)\n\t" \
    "buffer_load_dwordx4 a[216:219], v0, s[8:11], s42 offen\n\t" \
    "buffer_load_dwordx4 a[220:223], v0, s[8:11], s42 offen offset:1024\n\t" \
    "s_add_u32 s42, s42, 2048\n\t" \
    "s_waitcnt vmcnt(52)\n\t" \
    "buffer_load_dwordx4 a[224:227], v0, s[8:11], s42 offen\n\t" \
    "buffer_load_dwordx4 a[228:231], v0, s[8:11], s42 offen offset:1024\n\t" \
    "s_add_u32 s42, s42, 2048\n\t" \
    "s_waitcnt vmcnt(52)\n\t" \
    "buffer_load_dwordx4 a[232:235], v0, s[8:11], s42 offen\n\t" \
    "buffer_load_dwordx4 a[236:239], v0, s[8:11], s42 offen offset:1024\n\t" \
    "s_add_u32 s42, s42, 2048\n\t" \
    "s_waitcnt vmcnt(52)\n\t" \
    "buffer_load_dwordx4 a[240:243], v0, s[8:11], s42 offen\n\t" \
    "buffer_load_dwordx4 a[244:247], v0, s[8:11], s42 offen offset:1024\n\t" \
    "s_add_u32 s42, s42, 2048\n\t" \
    "s_waitcnt vmcnt(52)\n\t" \
    "buffer_load_dwordx4 a[248:251], v0, s[8:11], s42 offen\n\t" \
    "buffer_load_dwordx4 a[252:255], v0, s[8:11], s42 offen offset:1024\n\t" \
    "s_add_u32 s42, s42, 2048\n\t" \
    "s_waitcnt vmcnt(52)\n\t" \
    "buffer_load_dwordx4 v[232:235], v0, s[8:11], s42 offen\n\t" \
    "buffer_load_dwordx4 v[236:239], v0, s[8:11], s42 offen offset:1024\n\t" \
    "s_add_u32 s42, s42, 2048\n\t" \
    "s_waitcnt vmcnt(52)\n\t" \
    "buffer_load_dwordx4 v[240:243], v0, s[8:11], s42 offen\n\t" \
    "buffer_load_dwordx4 v[244:247], v0, s[8:11], s42 offen offset:1024\n\t" \
    "s_add_u32 s42, s42, 2048\n\t" \
    "s_waitcnt vmcnt(52)\n\t" \
    "buffer_load_dwordx4 v[248:251], v0, s[8:11], s42 offen\n\t" \
    "buffer_load_dwordx4 v[252:255], v0, s[8:11], s42 offen offset:1024\n\t" \
    "s_add_u32 s42, s42, 2048\n\t" \
    "s_waitcnt vmcnt(52)\n\t" \
    "s_waitcnt vmcnt(0)\n\t" \
    "s_mov_b32 s43, s14\n\t" \
    "s_mov_b32 s28, 0\n\t" \
    "s_mov_b32 s60, 0\n\t" \
    "s_mov_b32 s29, 0\n\t" \
    "s_mov_b32 s30, 0\n\t" \
    "s_mov_b32 s31, 0\n\t" \
    "s_mov_b32 s33, s15\n\t" \
    "s_add_u32 s34, s15, 4096\n\t" \
    "v_lshlrev_b32 v17, 1, v0\n\t" \
    "v_and_b32 v17, 0x780, v17\n\t" \
    "v_bfe_u32 v10, v0, 4, 2\n\t" \
    "v_lshl_or_b32 v17, v10, 2, v17\n\t" \
    "s_memrealtime s[58:59]\n\t" \
    "s_waitcnt lgkmcnt(0)\n\t" \
    "s_mov_b32 s59, 0\n\t" \
    "LP_loop_%=:\n\t" \
    "s_barrier\n\t" \
    "s_add_u32 s42, s28, 1\n\t" \
    "s_cmp_eq_u32 s42, 64\n\t" \
    "s_cbranch_scc1 LP_yskip_%=\n\t" \
    "s_cmp_gt_u32 s60, s28\n\t" \
    "s_cbranch_scc1 LP_yok_%=\n\t" \
    "s_mov_b32 s61, 0\n\t" \
    "LP_ywait_%=:\n\t" \
    "buffer_load_dword v10, v6, s[8:11], s16 offen sc1\n\t" \
    "s_waitcnt vmcnt(0)\n\t" \
    "s_nop 1\n\t" \
    "v_min_u32_dpp v10, v10, v10 row_ror:8 row_mask:0xf bank_mask:0xf\n\t" \
    "s_nop 1\n\t" \
    "v_min_u32_dpp v10, v10, v10 row_ror:4 row_mask:0xf bank_mask:0xf\n\t" \
    "s_nop 1\n\t" \
    "v_min_u32_dpp v10, v10, v10 row_ror:2 row_mask:0xf bank_mask:0xf\n\t" \
    "s_nop 1\n\t" \
    "v_min_u32_dpp v10, v10, v10 row_ror:1 row_mask:0xf bank_mask:0xf\n\t" \
    "s_nop 1\n\t" \
    "v_readfirstlane_b32 s60, v10\n\t" \
    "s_add_u32 s61, s61, 1\n\t" \
    "s_lshl_b32 s60, s60, 1\n\t" \
    "s_cmp_gt_u32 s60, s28\n\t" \
    "s_cbranch_scc1 LP_yok_%=\n\t" \
    "s_sleep 4\n\t" \
    "s_cmp_gt_u32 s61, 131072\n\t" \
    "s_cbranch_scc0 LP_ywait_%=\n\t" \
    "LP_yok_%=:\n\t" \
    "LP_yskip_%=:\n\t" \
    "s_add_u32 s30, s30, 1\n\t" \
    "s_lshr_b32 s42, s30, 5\n\t" \
    "s_lshl_b32 s42, s42, 2\n\t" \
    "s_and_b32 s45, s30, 1\n\t" \
    "s_lshl_b32 s45, s45, 1\n\t" \
    "s_add_u32 s42, s42, s45\n\t" \
    "s_lshr_b32 s42, s12, s42\n\t" \
    "s_and_b32 s42, s42, 3\n\t" \
    "s_lshl_b32 s42, s42, 13\n\t" \
    "s_lshr_b32 s45, s30, 1\n\t" \
    "s_lshl_b32 s45, s45, 15\n\t" \
    "s_add_u32 s36, s42, s45\n\t" \
    "s_add_u32 s30, s30, 1\n\t" \
    "s_lshr_b32 s42, s30, 5\n\t" \
    "s_lshl_b32 s42, s42, 2\n\t" \
    "s_and_b32 s45, s30, 1\n\t" \
    "s_lshl_b32 s45, s45, 1\n\t" \
    "s_add_u32 s42, s42, s45\n\t" \
    "s_lshr_b32 s42, s12, s42\n\t" \
    "s_and_b32 s42, s42, 3\n\t" \
    "s_lshl_b32 s42, s42, 13\n\t" \
    "s_lshr_b32 s45, s30, 1\n\t" \
    "s_lshl_b32 s45, s45, 15\n\t" \
    "s_add_u32 s37, s42, s45\n\t" \
    "s_add_u32 s30, s30, 1\n\t" \
    "s_lshr_b32 s42, s30, 5\n\t" \
    "s_lshl_b32 s42, s42, 2\n\t" \
    "s_and_b32 s45, s30, 1\n\t" \
    "s_lshl_b32 s45, s45, 1\n\t" \
    "s_add_u32 s42, s42, s45\n\t" \
    "s_lshr_b32 s42, s12, s42\n\t" \
    "s_and_b32 s42, s42, 3\n\t" \
    "s_lshl_b32 s42, s42, 13\n\t" \
    "s_lshr_b32 s45, s30, 1\n\t" \
    "s_lshl_b32 s45, s45, 15\n\t" \
    "s_add_u32 s35, s42, s45\n\t" \
    "s_add_u32 s38, s36, 4096\n\t" \
    "s_add_u32 s39, s37, 4096\n\t" \
    "s_add_u32 s62, s36, 5120\n\t" \
    "v_mov_b64 v[72:73], 0\n\t" \
    "v_mov_b64 v[74:75], 0\n\t" \
    "v_mov_b64 v[76:77], 0\n\t" \
    "v_mov_b64 v[78:79], 0\n\t" \
    "v_mov_b64 v[80:81], 0\n\t" \
    "v_mov_b64 v[82:83], 0\n\t" \
    "v_mov_b64 v[84:85], 0\n\t" \
    "v_mov_b64 v[86:87], 0\n\t" \
    "v_mov_b64 v[88:89], 0\n\t" \
    "v_mov_b64 v[90:91], 0\n\t" \
    "v_mov_b64 v[24:25], 0\n\t" \
    "v_mov_b64 v[26:27], 0\n\t" \
    "v_mov_b64 v[28:29], 0\n\t" \
    "v_mov_b64 v[30:31], 0\n\t" \
    "s_barrier\n\t" \
    "ds_read_b128 v[32:35], v5 offset:8192\n\t" \
    "ds_read_b128 v[36:39], v5 offset:9216\n\t" \
    "ds_read_b128 v[40:43], v5 offset:10240\n\t" \
    "ds_read_b128 v[44:47], v5 offset:11264\n\t" \
    "ds_read_b128 v[48:51], v5 offset:12288\n\t" \
    "ds_read_b128 v[52:55], v5 offset:13312\n\t" \
    "ds_read_b128 v[56:59], v5 offset:14336\n\t" \
    "s_waitcnt lgkmcnt(6)\n\t" \
    "v_smfmac_f32_16x16x64_bf16 v[72:75], v[32:35], a[0:7], v1\n\t" \
    "s_add_u32 s44, s43, 0\n\t" \
    "buffer_load_dwordx4 v[104:107], v0, s[8:11], s44 offen sc1\n\t" \
    "v_smfmac_f32_16x16x64_bf16 v[76:79], v[32:35], a[56:63], v1\n\t" \
    "buffer_load_dwordx4 v[108:111], v0, s[8:11], s44 offen offset:1024 sc1\n\t" \
    "v_smfmac_f32_16x16x64_bf16 v[80:83], v[32:35], a[112:119], v1\n\t" \
    "buffer_load_dwordx4 v[120:123], v0, s[8:11], s44 offen offset:2048 sc1\n\t" \
    "v_smfmac_f32_16x16x64_bf16 v[84:87], v[32:35], a[168:175], v1\n\t" \
    "buffer_load_dwordx4 v[124:127], v0, s[8:11], s44 offen offset:3072 sc1\n\t" \
    "v_smfmac_f32_16x16x64_bf16 v[88:91], v[32:35], a[224:231], v1\n\t" \
    "s_add_u32 s44, s43, 4096\n\t" \
    "buffer_load_dwordx4 v[136:139], v0, s[8:11], s44 offen sc1\n\t" \
    "s_waitcnt lgkmcnt(5)\n\t" \
    "v_smfmac_f32_16x16x64_bf16 v[72:75], v[36:39], a[8:15], v1\n\t" \
    "buffer_load_dwordx4 v[140:143], v0, s[8:11], s44 offen offset:1024 sc1\n\t" \
    "v_smfmac_f32_16x16x64_bf16 v[76:79], v[36:39], a[64:71], v1\n\t" \
    "buffer_load_dwordx4 v[152:155], v0, s[8:11], s44 offen offset:2048 sc1\n\t" \
    "v_smfmac_f32_16x16x64_bf16 v[80:83], v[36:39], a[120:127], v1\n\t" \
    "buffer_load_dwordx4 v[156:159], v0, s[8:11], s44 offen offset:3072 sc1\n\t" \
    "v_smfmac_f32_16x16x64_bf16 v[84:87], v[36:39], a[176:183], v1\n\t" \
    "s_add_u32 s44, s43, 8192\n\t" \
    "buffer_load_dwordx4 v[168:171], v0, s[8:11], s44 offen sc1\n\t" \
    "v_smfmac_f32_16x16x64_bf16 v[88:91], v[36:39], a[232:239], v1\n\t" \
    "buffer_load_dwordx4 v[172:175], v0, s[8:11], s44 offen offset:1024 sc1\n\t" \
    "s_waitcnt lgkmcnt(4)\n\t" \
    "v_smfmac_f32_16x16x64_bf16 v[72:75], v[40:43], a[16:23], v1\n\t" \
    "buffer_load_dwordx4 v[184:187], v0, s[8:11], s44 offen offset:2048 sc1\n\t" \
    "v_smfmac_f32_16x16x64_bf16 v[76:79], v[40:43], a[72:79], v1\n\t" \
    "buffer_load_dwordx4 v[188:191], v0, s[8:11], s44 offen offset:3072 sc1\n\t" \
    "v_smfmac_f32_16x16x64_bf16 v[80:83], v[40:43], a[128:135], v1\n\t" \
    "s_add_u32 s44, s43, 12288\n\t" \
    "buffer_load_dwordx4 v[200:203], v0, s[8:11], s44 offen sc1\n\t" \
    "v_smfmac_f32_16x16x64_bf16 v[84:87], v[40:43], a[184:191], v1\n\t" \
    "buffer_load_dwordx4 v[204:207], v0, s[8:11], s44 offen offset:1024 sc1\n\t" \
    "v_smfmac_f32_16x16x64_bf16 v[88:91], v[40:43], a[240:247], v1\n\t" \
    "buffer_load_dwordx4 v[216:219], v0, s[8:11], s44 offen offset:2048 sc1\n\t" \
    "buffer_load_dword v16, v17, s[8:11], s62 offen sc1\n\t" \
    "s_waitcnt lgkmcnt(3)\n\t" \
    "v_smfmac_f32_16x16x64_bf16 v[72:75], v[44:47], a[24:31], v1\n\t" \
    "buffer_load_dwordx4 v[220:223], v0, s[8:11], s44 offen offset:3072 sc1\n\t" \
    "v_smfmac_f32_16x16x64_bf16 v[76:79], v[44:47], a[80:87], v1\n\t" \
    "s_add_u32 s44, s43, 16384\n\t" \
    "buffer_load_dwordx4 v[112:115], v0, s[8:11], s44 offen sc1\n\t" \
    "v_smfmac_f32_16x16x64_bf16 v[80:83], v[44:47], a[136:143], v1\n\t" \
    "buffer_load_dwordx4 v[116:119], v0, s[8:11], s44 offen offset:1024 sc1\n\t" \
    "v_smfmac_f32_16x16x64_bf16 v[84:87], v[44:47], a[192:199], v1\n\t" \
    "buffer_load_dwordx4 v[128:131], v0, s[8:11], s44 offen offset:2048 sc1\n\t" \
    "v_smfmac_f32_16x16x64_bf16 v[88:91], v[44:47], a[248:255], v1\n\t" \
    "buffer_load_dwordx4 v[132:135], v0, s[8:11], s44 offen offset:3072 sc1\n\t" \
    "s_waitcnt lgkmcnt(2)\n\t" \
    "v_smfmac_f32_16x16x64_bf16 v[72:75], v[48:51], a[32:39], v1\n\t" \
    "s_add_u32 s44, s43, 20480\n\t" \
    "buffer_load_dwordx4 v[144:147], v0, s[8:11], s44 offen sc1\n\t" \
    "v_smfmac_f32_16x16x64_bf16 v[76:79], v[48:51], a[88:95], v1\n\t" \
    "buffer_load_dwordx4 v[148:151], v0, s[8:11], s44 offen offset:1024 sc1\n\t" \
    "v_smfmac_f32_16x16x64_bf16 v[80:83], v[48:51], a[144:151], v1\n\t" \
    "buffer_load_dwordx4 v[160:163], v0, s[8:11], s44 offen offset:2048 sc1\n\t" \
    "v_smfmac_f32_16x16x64_bf16 v[84:87], v[48:51], a[200:207], v1\n\t" \
    "buffer_load_dwordx4 v[164:167], v0, s[8:11], s44 offen offset:3072 sc1\n\t" \
    "v_smfmac_f32_16x16x64_bf16 v[88:91], v[48:51], v[232:239], v1\n\t" \
    "s_add_u32 s44, s43, 24576\n\t" \
    "buffer_load_dwordx4 v[176:179], v0, s[8:11], s44 offen sc1\n\t" \
    "s_waitcnt lgkmcnt(1)\n\t" \
    "v_smfmac_f32_16x16x64_bf16 v[72:75], v[52:55], a[40:47], v1\n\t" \
    "buffer_load_dwordx4 v[180:183], v0, s[8:11], s44 offen offset:1024 sc1\n\t" \
    "v_smfmac_f32_16x16x64_bf16 v[76:79], v[52:55], a[96:103], v1\n\t" \
    "buffer_load_dwordx4 v[192:195], v0, s[8:11], s44 offen offset:2048 sc1\n\t" \
    "v_smfmac_f32_16x16x64_bf16 v[80:83], v[52:55], a[152:159], v1\n\t" \
    "buffer_load_dwordx4 v[196:199], v0, s[8:11], s44 offen offset:3072 sc1\n\t" \
    "v_smfmac_f32_16x16x64_bf16 v[84:87], v[52:55], a[208:215], v1\n\t" \
    "s_add_u32 s44, s43, 28672\n\t" \
    "buffer_load_dwordx4 v[208:211], v0, s[8:11], s44 offen sc1\n\t" \
    "v_smfmac_f32_16x16x64_bf16 v[88:91], v[52:55], v[240:247], v1\n\t" \
    "buffer_load_dwordx4 v[212:215], v0, s[8:11], s44 offen offset:1024 sc1\n\t" \
    "s_waitcnt lgkmcnt(0)\n\t" \
    "v_smfmac_f32_16x16x64_bf16 v[72:75], v[56:59], a[48:55], v1\n\t" \
    "buffer_load_dwordx4 v[224:227], v0, s[8:11], s44 offen offset:2048 sc1\n\t" \
    "v_smfmac_f32_16x16x64_bf16 v[76:79], v[56:59], a[104:111], v1\n\t" \
    "buffer_load_dwordx4 v[228:231], v0, s[8:11], s44 offen offset:3072 sc1\n\t" \
    "v_smfmac_f32_16x16x64_bf16 v[80:83], v[56:59], a[160:167], v1\n\t" \
    "v_smfmac_f32_16x16x64_bf16 v[84:87], v[56:59], a[216:223], v1\n\t" \
    "v_smfmac_f32_16x16x64_bf16 v[88:91], v[56:59], v[248:255], v1\n\t" \
    "s_add_u32 s43, s43, s20\n\t" \
    "s_nop 7\n\t" \
    "v_add_f32 v72, v72, v73\n\t" \
    "v_add_f32 v74, v74, v75\n\t" \
    "v_add_f32 v76, v76, v77\n\t" \
    "v_add_f32 v78, v78, v79\n\t" \
    "v_add_f32 v80, v80, v81\n\t" \
    "v_add_f32 v82, v82, v83\n\t" \
    "v_add_f32 v84, v84, v85\n\t" \
    "v_add_f32 v86, v86, v87\n\t" \
    "v_add_f32 v88, v88, v89\n\t" \
    "v_add_f32 v90, v90, v91\n\t" \
    "v_cvt_pk_bf16_f32 v72, v72, v74\n\t" \
    "v_cvt_pk_bf16_f32 v76, v76, v78\n\t" \
    "v_cvt_pk_bf16_f32 v80, v80, v82\n\t" \
    "v_cvt_pk_bf16_f32 v84, v84, v86\n\t" \
    "v_cvt_pk_bf16_f32 v88, v88, v90\n\t" \
    "ds_write_b32 v3, v72 offset:0\n\t" \
    "ds_write_b32 v3, v76 offset:2048\n\t" \
    "ds_write_b32 v3, v80 offset:4\n\t" \
    "ds_write_b32 v3, v84 offset:2052\n\t" \
    "ds_write_b32 v3, v88 offset:512\n\t" \
    "s_add_u32 s28, s28, 1\n\t" \
    "s_cmp_eq_u32 s28, 64\n\t" \
    "s_cbranch_scc1 LP_lastz_%=\n\t" \
    "s_mov_b32 s61, 0\n\t" \
    "s_waitcnt vmcnt(17)\n\t" \
    "LP_schk_%=:\n\t" \
    "v_cmp_eq_u32 vcc, -1, v16\n\t" \
    "s_add_u32 s61, s61, 1\n\t" \
    "s_cmp_gt_u32 s61, 131072\n\t" \
    "s_cbranch_scc1 LP_sok_%=\n\t" \
    "s_cbranch_vccz LP_sok_%=\n\t" \
    "buffer_load_dword v16, v17, s[8:11], s62 offen sc1\n\t" \
    "s_waitcnt vmcnt(0)\n\t" \
    "s_branch LP_schk_%=\n\t" \
    "LP_sok_%=:\n\t" \
    "v_subrev_u32 v16, s58, v16\n\t" \
    "s_nop 1\n\t" \
    "v_max_i32_dpp v16, v16, v16 row_ror:8 row_mask:0xf bank_mask:0xf\n\t" \
    "s_nop 1\n\t" \
    "v_max_i32_dpp v16, v16, v16 row_ror:4 row_mask:0xf bank_mask:0xf\n\t" \
    "s_nop 1\n\t" \
    "v_max_i32_dpp v16, v16, v16 row_ror:2 row_mask:0xf bank_mask:0xf\n\t" \
    "s_nop 1\n\t" \
    "v_max_i32_dpp v16, v16, v16 row_ror:1 row_mask:0xf bank_mask:0xf\n\t" \
    "s_nop 1\n\t" \
    "v_readlane_b32 s42, v16, 0\n\t" \
    "v_readlane_b32 s45, v16, 16\n\t" \
    "s_max_i32 s42, s42, s45\n\t" \
    "v_readlane_b32 s45, v16, 32\n\t" \
    "v_readlane_b32 s63, v16, 48\n\t" \
    "s_max_i32 s45, s45, s63\n\t" \
    "s_max_i32 s42, s42, s45\n\t" \
    "s_add_u32 s58, s58, s42\n\t" \
    "s_add_u32 s63, s58, s23\n\t" \
    "LP_pw_%=:\n\t" \
    "s_memrealtime s[56:57]\n\t" \
    "s_waitcnt lgkmcnt(0)\n\t" \
    "s_sub_u32 s42, s63, s56\n\t" \
    "s_cmp_gt_i32 s42, 400\n\t" \
    "s_cselect_b32 s42, 0, s42\n\t" \
    "s_cmp_gt_i32 s42, 6\n\t" \
    "s_cbranch_scc1 LP_pw_%=\n\t" \
    "s_mul_i32 s42, s42, 3\n\t" \
    "s_sub_u32 s42, s42, 8\n\t" \
    "s_max_i32 s42, s42, 0\n\t" \
    "s_min_i32 s42, s42, 16\n\t" \
    "s_lshl_b32 s42, s42, 2\n\t" \
    "s_sub_u32 s42, 76, s42\n\t" \
    "s_getpc_b64 s[40:41]\n\t" \
    "s_add_u32 s40, s40, s42\n\t" \
    "s_addc_u32 s41, s41, 0\n\t" \
    "s_setpc_b64 s[40:41]\n\t" \
    ".rept 16\n\t" \
    "s_nop 1\n\t" \
    ".endr\n\t" \
    "buffer_load_dwordx4 v[40:43], v0, s[8:11], s36 offen offset:2048 sc1\n\t" \
    "buffer_load_dwordx4 v[44:47], v0, s[8:11], s36 offen offset:3072 sc1\n\t" \
    "buffer_load_dwordx4 v[48:51], v0, s[8:11], s38 offen sc1\n\t" \
    "buffer_load_dwordx4 v[52:55], v0, s[8:11], s37 offen sc1\n\t" \
    "buffer_load_dwordx4 v[56:59], v0, s[8:11], s37 offen offset:1024 sc1\n\t" \
    "buffer_load_dwordx4 v[60:63], v0, s[8:11], s37 offen offset:2048 sc1\n\t" \
    "buffer_load_dwordx4 v[64:67], v0, s[8:11], s37 offen offset:3072 sc1\n\t" \
    "buffer_load_dwordx4 v[68:71], v0, s[8:11], s39 offen sc1\n\t" \
    "buffer_load_dwordx4 v[32:35], v0, s[8:11], s36 offen sc1\n\t" \
    "buffer_load_dwordx4 v[36:39], v0, s[8:11], s36 offen offset:1024 sc1\n\t" \
    "s_barrier\n\t" \
    "ds_read_b128 v[72:75], v5 offset:0\n\t" \
    "ds_read_b128 v[76:79], v5 offset:1024\n\t" \
    "ds_read_b128 v[80:83], v5 offset:2048\n\t" \
    "ds_read_b128 v[84:87], v5 offset:3072\n\t" \
    "ds_read_b128 v[88:91], v5 offset:4096\n\t" \
    "ds_read_b128 v[92:95], v5 offset:5120\n\t" \
    "ds_read_b128 v[96:99], v5 offset:6144\n\t" \
    "ds_read_b128 v[100:103], v5 offset:7168\n\t" \
    "LP_pgo_%=:\n\t" \
    "s_waitcnt vmcnt(9)\n\t" \
    "v_smfmac_f32_16x16x64_bf16 v[24:27], v[40:43], v[104:111], v1\n\t" \
    "v_max3_u32 v10, v40, v41, v42\n\t" \
    "v_max_u32 v10, v10, v43\n\t" \
    "v_smfmac_f32_16x16x64_bf16 v[28:31], v[40:43], v[112:119], v1\n\t" \
    "s_waitcnt vmcnt(8)\n\t" \
    "v_smfmac_f32_16x16x64_bf16 v[24:27], v[44:47], v[120:127], v1\n\t" \
    "v_max3_u32 v10, v44, v45, v10\n\t" \
    "v_max3_u32 v10, v46, v47, v10\n\t" \
    "v_cmp_eq_u32 vcc, -1, v10\n\t" \
    "v_smfmac_f32_16x16x64_bf16 v[28:31], v[44:47], v[128:135], v1\n\t" \
    "s_cbranch_vccnz LP_pfail_%=\n\t" \
    "s_waitcnt vmcnt(7)\n\t" \
    "v_smfmac_f32_16x16x64_bf16 v[24:27], v[48:51], v[136:143], v1\n\t" \
    "v_max3_u32 v10, v48, v49, v50\n\t" \
    "v_max_u32 v10, v10, v51\n\t" \
    "v_smfmac_f32_16x16x64_bf16 v[28:31], v[48:51], v[144:151], v1\n\t" \
    "s_waitcnt vmcnt(6)\n\t" \
    "v_smfmac_f32_16x16x64_bf16 v[24:27], v[52:55], v[152:159], v1\n\t" \
    "v_max3_u32 v10, v52, v53, v10\n\t" \
    "v_max3_u32 v10, v54, v55, v10\n\t" \
    "v_smfmac_f32_16x16x64_bf16 v[28:31], v[52:55], v[160:167], v1\n\t" \
    "s_waitcnt vmcnt(5)\n\t" \
    "v_smfmac_f32_16x16x64_bf16 v[24:27], v[56:59], v[168:175], v1\n\t" \
    "v_max3_u32 v10, v56, v57, v10\n\t" \
    "v_max3_u32 v10, v58, v59, v10\n\t" \
    "v_cmp_eq_u32 vcc, -1, v10\n\t" \
    "v_smfmac_f32_16x16x64_bf16 v[28:31], v[56:59], v[176:183], v1\n\t" \
    "s_cbranch_vccnz LP_pfail_%=\n\t" \
    "s_waitcnt vmcnt(4)\n\t" \
    "v_smfmac_f32_16x16x64_bf16 v[24:27], v[60:63], v[184:191], v1\n\t" \
    "v_max3_u32 v10, v60, v61, v62\n\t" \
    "v_max_u32 v10, v10, v63\n\t" \
    "v_smfmac_f32_16x16x64_bf16 v[28:31], v[60:63], v[192:199], v1\n\t" \
    "s_waitcnt vmcnt(3)\n\t" \
    "v_smfmac_f32_16x16x64_bf16 v[24:27], v[64:67], v[200:207], v1\n\t" \
    "v_max3_u32 v10, v64, v65, v10\n\t" \
    "v_max3_u32 v10, v66, v67, v10\n\t" \
    "v_smfmac_f32_16x16x64_bf16 v[28:31], v[64:67], v[208:215], v1\n\t" \
    "s_waitcnt vmcnt(2)\n\t" \
    "v_smfmac_f32_16x16x64_bf16 v[24:27], v[68:71], v[216:223], v1\n\t" \
    "v_max3_u32 v10, v68, v69, v10\n\t" \
    "v_max3_u32 v10, v70, v71, v10\n\t" \
    "v_smfmac_f32_16x16x64_bf16 v[28:31], v[68:71], v[224:231], v1\n\t" \
    "s_waitcnt vmcnt(1)\n\t" \
    "s_waitcnt lgkmcnt(0)\n\t" \
    "v_smfmac_f32_16x16x64_bf16 v[24:27], v[32:35], v[72:79], v1\n\t" \
    "v_max3_u32 v10, v32, v33, v10\n\t" \
    "v_max3_u32 v10, v34, v35, v10\n\t" \
    "v_smfmac_f32_16x16x64_bf16 v[28:31], v[32:35], v[80:87], v1\n\t" \
    "s_waitcnt vmcnt(0)\n\t" \
    "v_smfmac_f32_16x16x64_bf16 v[24:27], v[36:39], v[88:95], v1\n\t" \
    "v_max3_u32 v10, v36, v37, v10\n\t" \
    "v_max3_u32 v10, v38, v39, v10\n\t" \
    "v_cmp_eq_u32 vcc, -1, v10\n\t" \
    "v_smfmac_f32_16x16x64_bf16 v[28:31], v[36:39], v[96:103], v1\n\t" \
    "s_cbranch_vccnz LP_pfail_%=\n\t" \
    "s_nop 4\n\t" \
    "v_add_f32 v24, v24, v25\n\t" \
    "v_add_f32 v26, v26, v27\n\t" \
    "v_add_f32 v28, v28, v29\n\t" \
    "v_add_f32 v30, v30, v31\n\t" \
    "v_cvt_pk_bf16_f32 v24, v24, v28\n\t" \
    "v_cvt_pk_bf16_f32 v26, v26, v30\n\t" \
    "v_min_u32 v24, -2, v24\n\t" \
    "v_min_u32 v26, -2, v26\n\t" \
    "buffer_store_dword v24, v2, s[8:11], s35 offen sc1\n\t" \
    "buffer_store_dword v26, v2, s[8:11], s35 offen offset:32 sc1\n\t" \
    "s_mov_b32 s29, 0\n\t" \
    "s_waitcnt lgkmcnt(0)\n\t" \
    "s_branch LP_loop_%=\n\t" \
    "LP_pfail_%=:\n\t" \
    "s_add_u32 s29, s29, 1\n\t" \
    "s_cmp_gt_u32 s29, 131072\n\t" \
    "s_cbranch_scc1 LP_end_%=\n\t" \
    "buffer_load_dwordx4 v[40:43], v0, s[8:11], s36 offen offset:2048 sc1\n\t" \
    "buffer_load_dwordx4 v[44:47], v0, s[8:11], s36 offen offset:3072 sc1\n\t" \
    "buffer_load_dwordx4 v[48:51], v0, s[8:11], s38 offen sc1\n\t" \
    "buffer_load_dwordx4 v[52:55], v0, s[8:11], s37 offen sc1\n\t" \
    "buffer_load_dwordx4 v[56:59], v0, s[8:11], s37 offen offset:1024 sc1\n\t" \
    "buffer_load_dwordx4 v[60:63], v0, s[8:11], s37 offen offset:2048 sc1\n\t" \
    "buffer_load_dwordx4 v[64:67], v0, s[8:11], s37 offen offset:3072 sc1\n\t" \
    "buffer_load_dwordx4 v[68:71], v0, s[8:11], s39 offen sc1\n\t" \
    "buffer_load_dwordx4 v[32:35], v0, s[8:11], s36 offen sc1\n\t" \
    "buffer_load_dwordx4 v[36:39], v0, s[8:11], s36 offen offset:1024 sc1\n\t" \
    "s_nop 7\n\t" \
    "v_mov_b64 v[24:25], 0\n\t" \
    "v_mov_b64 v[26:27], 0\n\t" \
    "v_mov_b64 v[28:29], 0\n\t" \
    "v_mov_b64 v[30:31], 0\n\t" \
    "s_branch LP_pgo_%=\n\t" \
    "LP_lastz_%=:\n\t" \
    "s_waitcnt lgkmcnt(0)\n\t" \
    "s_barrier\n\t" \
    "LP_end_%=:\n\t" \
    "s_waitcnt vmcnt(0)\n\t" \
    "s_barrier\n\t" \
    "s_endpgm\n\t"
#define ASM_CHAIN_Z1 \
    "s_nop 7\n\t" \
    "s_mov_b32 s42, s13\n\t" \
    "buffer_load_dwordx4 a[0:3], v0, s[8:11], s42 offen\n\t" \
    "buffer_load_dwordx4 a[4:7], v0, s[8:11], s42 offen offset:1024\n\t" \
    "s_add_u32 s42, s42, 2048\n\t" \
    "buffer_load_dwordx4 a[8:11], v0, s[8:11], s42 offen\n\t" \
    "buffer_load_dwordx4 a[12:15], v0, s[8:11], s42 offen offset:1024\n\t" \
    "s_add_u32 s42, s42, 2048\n\t" \
    "buffer_load_dwordx4 a[16:19], v0, s[8:11], s42 offen\n\t" \
    "buffer_load_dwordx4 a[20:23], v0, s[8:11], s42 offen offset:1024\n\t" \
    "s_add_u32 s42, s42, 2048\n\t" \
    "buffer_load_dwordx4 a[24:27], v0, s[8:11], s42 offen\n\t" \
    "buffer_load_dwordx4 a[28:31], v0, s[8:11], s42 offen offset:1024\n\t" \
    "s_add_u32 s42, s42, 2048\n\t" \
    "buffer_load_dwordx4 a[32:35], v0, s[8:11], s42 offen\n\t" \
    "buffer_load_dwordx4 a[36:39], v0, s[8:11], s42 offen offset:1024\n\t" \
    "s_add_u32 s42, s42, 2048\n\t" \
    "buffer_load_dwordx4 a[40:43], v0, s[8:11], s42 offen\n\t" \
    "buffer_load_dwordx4 a[44:47], v0, s[8:11], s42 offen offset:1024\n\t" \
    "s_add_u32 s42, s42, 2048\n\t" \
    "buffer_load_dwordx4 a[48:51], v0, s[8:11], s42 offen\n\t" \
    "buffer_load_dwordx4 a[52:55], v0, s[8:11], s42 offen offset:1024\n\t" \
    "s_add_u32 s42, s42, 2048\n\t" \
    "buffer_load_dwordx4 a[56:59], v0, s[8:11], s42 offen\n\t" \
    "buffer_load_dwordx4 a[60:63], v0, s[8:11], s42 offen offset:1024\n\t" \
    "s_add_u32 s42, s42, 2048\n\t" \
    "buffer_load_dwordx4 a[64:67], v0, s[8:11], s42 offen\n\t" \
    "buffer_load_dwordx4 a[68:71], v0, s[8:11], s42 offen offset:1024\n\t" \
    "s_add_u32 s42, s42, 2048\n\t" \
    "buffer_load_dwordx4 a[72:75], v0, s[8:11], s42 offen\n\t" \
    "buffer_load_dwordx4 a[76:79], v0, s[8:11], s42 offen offset:1024\n\t" \
    "s_add_u32 s42, s42, 2048\n\t" \
    "buffer_load_dwordx4 a[80:83], v0, s[8:11], s42 offen\n\t" \
    "buffer_load_dwordx4 a[84:87], v0, s[8:11], s42 offen offset:1024\n\t" \
    "s_add_u32 s42, s42, 2048\n\t" \
    "buffer_load_dwordx4 a[88:91], v0, s[8:11], s42 offen\n\t" \
    "buffer_load_dwordx4 a[92:95], v0, s[8:11], s42 offen offset:1024\n\t" \
    "s_add_u32 s42, s42, 2048\n\t" \
    "buffer_load_dwordx4 a[96:99], v0, s[8:11], s42 offen\n\t" \
    "buffer_load_dwordx4 a[100:103], v0, s[8:11], s42 offen offset:1024\n\t" \
    "s_add_u32 s42, s42, 2048\n\t" \
    "buffer_load_dwordx4 a[104:107], v0, s[8:11], s42 offen\n\t" \
    "buffer_load_dwordx4 a[108:111], v0, s[8:11], s42 offen offset:1024\n\t" \
    "s_add_u32 s42, s42, 2048\n\t" \
    "buffer_load_dwordx4 a[112:115], v0, s[8:11], s42 offen\n\t" \
    "buffer_load_dwordx4 a[116:119], v0, s[8:11], s42 offen offset:1024\n\t" \
    "s_add_u32 s42, s42, 2048\n\t" \
    "buffer_load_dwordx4 a[120:123], v0, s[8:11], s42 offen\n\t" \
    "buffer_load_dwordx4 a[124:127], v0, s[8:11], s42 offen offset:1024\n\t" \
    "s_add_u32 s42, s42, 2048\n\t" \
    "buffer_load_dwordx4 a[128:131], v0, s[8:11], s42 offen\n\t" \
    "buffer_load_dwordx4 a[132:135], v0, s[8:11], s42 offen offset:1024\n\t" \
    "s_add_u32 s42, s42, 2048\n\t" \
    "buffer_load_dwordx4 a[136:139], v0, s[8:11], s42 offen\n\t" \
    "buffer_load_dwordx4 a[140:143], v0, s[8:11], s42 offen offset:1024\n\t" \
    "s_add_u32 s42, s42, 2048\n\t" \
    "buffer_load_dwordx4 a[144:147], v0, s[8:11], s42 offen\n\t" \
    "buffer_load_dwordx4 a[148:151], v0, s[8:11], s42 offen offset:1024\n\t" \
    "s_add_u32 s42, s42, 2048\n\t" \
    "buffer_load_dwordx4 a[152:155], v0, s[8:11], s42 offen\n\t" \
    "buffer_load_dwordx4 a[156:159], v0, s[8:11], s42 offen offset:1024\n\t" \
    "s_add_u32 s42, s42, 2048\n\t" \
    "buffer_load_dwordx4 a[160:163], v0, s[8:11], s42 offen\n\t" \
    "buffer_load_dwordx4 a[164:167], v0, s[8:11], s42 offen offset:1024\n\t" \
    "s_add_u32 s42, s42, 2048\n\t" \
    "buffer_load_dwordx4 a[168:171], v0, s[8:11], s42 offen\n\t" \
    "buffer_load_dwordx4 a[172:175], v0, s[8:11], s42 offen offset:1024\n\t" \
    "s_add_u32 s42, s42, 2048\n\t" \
    "buffer_load_dwordx4 a[176:179], v0, s[8:11], s42 offen\n\t" \
    "buffer_load_dwordx4 a[180:183], v0, s[8:11], s42 offen offset:1024\n\t" \
    "s_add_u32 s42, s42, 2048\n\t" \
    "buffer_load_dwordx4 a[184:187], v0, s[8:11], s42 offen\n\t" \
    "buffer_load_dwordx4 a[188:191], v0, s[8:11], s42 offen offset:1024\n\t" \
    "s_add_u32 s42, s42, 2048\n\t" \
    "buffer_load_dwordx4 a[192:195], v0, s[8:11], s42 offen\n\t" \
    "buffer_load_dwordx4 a[196:199], v0, s[8:11], s42 offen offset:1024\n\t" \
    "s_add_u32 s42, s42, 2048\n\t" \
    "buffer_load_dwordx4 a[200:203], v0, s[8:11], s42 offen\n\t" \
    "buffer_load_dwordx4 a[204:207], v0, s[8:11], s42 offen offset:1024\n\t" \
    "s_add_u32 s42, s42, 2048\n\t" \
    "buffer_load_dwordx4 a[208:211], v0, s[8:11], s42 offen\n\t" \
    "buffer_load_dwordx4 a[212:215], v0, s[8:11], s42 offen offset:1024\n\t" \
    "s_add_u32 s42, s42, 2048\n\t" \
    "s_waitcnt vmcnt(52)\n\t" \
    "buffer_load_dwordx4 a[216:219], v0, s[8:11], s42 offen\n\t" \
    "buffer_load_dwordx4 a[220:223], v0, s[8:11], s42 offen offset:1024\n\t" \
    "s_add_u32 s42, s42, 2048\n\t" \
    "s_waitcnt vmcnt(52)\n\t" \
    "buffer_load_dwordx4 a[224:227], v0, s[8:11], s42 offen\n\t" \
    "buffer_load_dwordx4 a[228:231], v0, s[8:11], s42 offen offset:1024\n\t" \
    "s_add_u32 s42, s42, 2048\n\t" \
    "s_waitcnt vmcnt(52)\n\t" \
    "buffer_load_dwordx4 a[232:235], v0, s[8:11], s42 offen\n\t" \
    "buffer_load_dwordx4 a[236:239], v0, s[8:11], s42 offen offset:1024\n\t" \
    "s_add_u32 s42, s42, 2048\n\t" \
    "s_waitcnt vmcnt(52)\n\t" \
    "buffer_load_dwordx4 a[240:243], v0, s[8:11], s42 offen\n\t" \
    "buffer_load_dwordx4 a[244:247], v0, s[8:11], s42 offen offset:1024\n\t" \
    "s_add_u32 s42, s42, 2048\n\t" \
    "s_waitcnt vmcnt(52)\n\t" \
    "buffer_load_dwordx4 a[248:251], v0, s[8:11], s42 offen\n\t" \
    "buffer_load_dwordx4 a[252:255], v0, s[8:11], s42 offen offset:1024\n\t" \
    "s_add_u32 s42, s42, 2048\n\t" \
    "s_waitcnt vmcnt(52)\n\t" \
    "buffer_load_dwordx4 v[120:123], v0, s[8:11], s42 offen\n\t" \
    "buffer_load_dwordx4 v[124:127], v0, s[8:11], s42 offen offset:1024\n\t" \
    "s_add_u32 s42, s42, 2048\n\t" \
    "s_waitcnt vmcnt(52)\n\t" \
    "buffer_load_dwordx4 v[128:131], v0, s[8:11], s42 offen\n\t" \
    "buffer_load_dwordx4 v[132:135], v0, s[8:11], s42 offen offset:1024\n\t" \
    "s_add_u32 s42, s42, 2048\n\t" \
    "s_waitcnt vmcnt(52)\n\t" \
    "buffer_load_dwordx4 v[136:139], v0, s[8:11], s42 offen\n\t" \
    "buffer_load_dwordx4 v[140:143], v0, s[8:11], s42 offen offset:1024\n\t" \
    "s_add_u32 s42, s42, 2048\n\t" \
    "s_waitcnt vmcnt(52)\n\t" \
    "buffer_load_dwordx4 v[144:147], v0, s[8:11], s42 offen\n\t" \
    "buffer_load_dwordx4 v[148:151], v0, s[8:11], s42 offen offset:1024\n\t" \
    "s_add_u32 s42, s42, 2048\n\t" \
    "s_waitcnt vmcnt(52)\n\t" \
    "buffer_load_dwordx4 v[152:155], v0, s[8:11], s42 offen\n\t" \
    "buffer_load_dwordx4 v[156:159], v0, s[8:11], s42 offen offset:1024\n\t" \
    "s_add_u32 s42, s42, 2048\n\t" \
    "s_waitcnt vmcnt(52)\n\t" \
    "buffer_load_dwordx4 v[160:163], v0, s[8:11], s42 offen\n\t" \
    "buffer_load_dwordx4 v[164:167], v0, s[8:11], s42 offen offset:1024\n\t" \
    "s_add_u32 s42, s42, 2048\n\t" \
    "s_waitcnt vmcnt(52)\n\t" \
    "buffer_load_dwordx4 v[168:171], v0, s[8:11], s42 offen\n\t" \
    "buffer_load_dwordx4 v[172:175], v0, s[8:11], s42 offen offset:1024\n\t" \
    "s_add_u32 s42, s42, 2048\n\t" \
    "s_waitcnt vmcnt(52)\n\t" \
    "buffer_load_dwordx4 v[176:179], v0, s[8:11], s42 offen\n\t" \
    "buffer_load_dwordx4 v[180:183], v0, s[8:11], s42 offen offset:1024\n\t" \
    "s_add_u32 s42, s42, 2048\n\t" \
    "s_waitcnt vmcnt(52)\n\t" \
    "buffer_load_dwordx4 v[184:187], v0, s[8:11], s42 offen\n\t" \
    "buffer_load_dwordx4 v[188:191], v0, s[8:11], s42 offen offset:1024\n\t" \
    "s_add_u32 s42, s42, 2048\n\t" \
    "s_waitcnt vmcnt(52)\n\t" \
    "buffer_load_dwordx4 v[192:195], v0, s[8:11], s42 offen\n\t" \
    "buffer_load_dwordx4 v[196:199], v0, s[8:11], s42 offen offset:1024\n\t" \
    "s_add_u32 s42, s42, 2048\n\t" \
    "s_waitcnt vmcnt(52)\n\t" \
    "buffer_load_dwordx4 v[200:203], v0, s[8:11], s42 offen\n\t" \
    "buffer_load_dwordx4 v[204:207], v0, s[8:11], s42 offen offset:1024\n\t" \
    "s_add_u32 s42, s42, 2048\n\t" \
    "s_waitcnt vmcnt(52)\n\t" \
    "buffer_load_dwordx4 v[208:211], v0, s[8:11], s42 offen\n\t" \
    "buffer_load_dwordx4 v[212:215], v0, s[8:11], s42 offen offset:1024\n\t" \
    "s_add_u32 s42, s42, 2048\n\t" \
    "s_waitcnt vmcnt(52)\n\t" \
    "buffer_load_dwordx4 v[216:219], v0, s[8:11], s42 offen\n\t" \
    "buffer_load_dwordx4 v[220:223], v0, s[8:11], s42 offen offset:1024\n\t" \
    "s_add_u32 s42, s42, 2048\n\t" \
    "s_waitcnt vmcnt(52)\n\t" \
    "buffer_load_dwordx4 v[224:227], v0, s[8:11], s42 offen\n\t" \
    "buffer_load_dwordx4 v[228:231], v0, s[8:11], s42 offen offset:1024\n\t" \
    "s_add_u32 s42, s42, 2048\n\t" \
    "s_waitcnt vmcnt(52)\n\t" \
    "buffer_load_dwordx4 v[232:235], v0, s[8:11], s42 offen\n\t" \
    "buffer_load_dwordx4 v[236:239], v0, s[8:11], s42 offen offset:1024\n\t" \
    "s_add_u32 s42, s42, 2048\n\t" \
    "s_waitcnt vmcnt(52)\n\t" \
    "buffer_load_dwordx4 v[240:243], v0, s[8:11], s42 offen\n\t" \
    "buffer_load_dwordx4 v[244:247], v0, s[8:11], s42 offen offset:1024\n\t" \
    "s_add_u32 s42, s42, 2048\n\t" \
    "s_waitcnt vmcnt(52)\n\t" \
    "buffer_load_dwordx4 v[248:251], v0, s[8:11], s42 offen\n\t" \
    "buffer_load_dwordx4 v[252:255], v0, s[8:11], s42 offen offset:1024\n\t" \
    "s_add_u32 s42, s42, 2048\n\t" \
    "s_waitcnt vmcnt(52)\n\t" \
    "s_waitcnt vmcnt(0)\n\t" \
    "s_mov_b32 s28, 0\n\t" \
    "s_mov_b32 s31, 0\n\t" \
    "s_mov_b32 s30, 0\n\t" \
    "s_mov_b32 s33, s15\n\t" \
    "s_add_u32 s34, s15, 4096\n\t" \
    "LZ1_loop_%=:\n\t" \
    "s_lshr_b32 s42, s30, 5\n\t" \
    "s_lshl_b32 s42, s42, 2\n\t" \
    "s_and_b32 s45, s30, 1\n\t" \
    "s_lshl_b32 s45, s45, 1\n\t" \
    "s_add_u32 s42, s42, s45\n\t" \
    "s_lshr_b32 s42, s12, s42\n\t" \
    "s_and_b32 s42, s42, 3\n\t" \
    "s_lshl_b32 s42, s42, 13\n\t" \
    "s_lshr_b32 s45, s30, 1\n\t" \
    "s_lshl_b32 s45, s45, 15\n\t" \
    "s_add_u32 s35, s42, s45\n\t" \
    "s_add_u32 s30, s30, 3\n\t" \
    "s_add_u32 s44, s35, 0\n\t" \
    "s_barrier\n\t" \
    "ds_read_b128 v[80:83], v5 offset:0\n\t" \
    "ds_read_b128 v[84:87], v5 offset:1024\n\t" \
    "ds_read_b128 v[88:91], v5 offset:14336\n\t" \
    "ds_read_b128 v[92:95], v5 offset:15360\n\t" \
    "ds_read_b128 v[96:99], v5 offset:2048\n\t" \
    "ds_read_b128 v[100:103], v5 offset:3072\n\t" \
    "ds_read_b128 v[104:107], v5 offset:16384\n\t" \
    "ds_read_b128 v[108:111], v5 offset:17408\n\t" \
    "v_mov_b64 v[44:45], 0\n\t" \
    "v_mov_b64 v[46:47], 0\n\t" \
    "v_mov_b64 v[48:49], 0\n\t" \
    "v_mov_b64 v[50:51], 0\n\t" \
    "v_mov_b64 v[52:53], 0\n\t" \
    "v_mov_b64 v[54:55], 0\n\t" \
    "v_mov_b64 v[56:57], 0\n\t" \
    "v_mov_b64 v[58:59], 0\n\t" \
    "v_mov_b64 v[60:61], 0\n\t" \
    "v_mov_b64 v[62:63], 0\n\t" \
    "v_mov_b64 v[64:65], 0\n\t" \
    "v_mov_b64 v[66:67], 0\n\t" \
    "v_mov_b64 v[68:69], 0\n\t" \
    "v_mov_b64 v[70:71], 0\n\t" \
    "v_mov_b64 v[72:73], 0\n\t" \
    "v_mov_b64 v[74:75], 0\n\t" \
    "v_mov_b64 v[76:77], 0\n\t" \
    "v_mov_b64 v[78:79], 0\n\t" \
    "s_mov_b32 s42, s55\n\t" \
    "s_max_i32 s42, s42, 0\n\t" \
    "s_min_i32 s42, s42, 384\n\t" \
    "s_lshl_b32 s42, s42, 2\n\t" \
    "s_sub_u32 s42, 1548, s42\n\t" \
    "s_getpc_b64 s[40:41]\n\t" \
    "s_add_u32 s40, s40, s42\n\t" \
    "s_addc_u32 s41, s41, 0\n\t" \
    "s_setpc_b64 s[40:41]\n\t" \
    ".rept 384\n\t" \
    "s_nop 1\n\t" \
    ".endr\n\t" \
    "LZ1_retry_%=:\n\t" \
    "buffer_load_dwordx4 v[16:19], v0, s[8:11], s44 offen sc1\n\t" \
    "buffer_load_dwordx4 v[20:23], v0, s[8:11], s44 offen offset:1024 sc1\n\t" \
    "buffer_load_dwordx4 v[24:27], v0, s[8:11], s44 offen offset:2048 sc1\n\t" \
    "s_waitcnt vmcnt(2)\n\t" \
    "ds_write_b128 v6, v[16:19] offset:8192\n\t" \
    "v_max3_u32 v10, v16, v17, v18\n\t" \
    "v_max_u32 v10, v10, v19\n\t" \
    "s_waitcnt vmcnt(1)\n\t" \
    "ds_write_b128 v6, v[20:23] offset:9216\n\t" \
    "v_max3_u32 v10, v20, v21, v10\n\t" \
    "v_max3_u32 v10, v22, v23, v10\n\t" \
    "s_waitcnt vmcnt(0)\n\t" \
    "ds_write_b128 v6, v[24:27] offset:10240\n\t" \
    "v_max3_u32 v10, v24, v25, v10\n\t" \
    "v_max3_u32 v10, v26, v27, v10\n\t" \
    "v_cmp_eq_u32 vcc, -1, v10\n\t" \
    "s_nop 1\n\t" \
    "s_cbranch_vccnz LZ1_fail_%=\n\t" \
    "s_waitcnt lgkmcnt(0)\n\t" \
    "s_barrier\n\t" \
    "v_mov_b32 v10, s28\n\t" \
    "s_mov_b64 exec, 1\n\t" \
    "buffer_store_dword v10, off, s[8:11], s17 sc1\n\t" \
    "s_mov_b64 exec, -1\n\t" \
    "ds_read_b128 v[28:31], v6 offset:11264\n\t" \
    "ds_read_b128 v[32:35], v6 offset:12288\n\t" \
    "ds_read_b128 v[36:39], v6 offset:13312\n\t" \
    "ds_read_b128 v[40:43], v6 offset:14336\n\t" \
    "v_smfmac_f32_16x16x64_bf16 v[44:47], v[16:19], a[0:7], v1\n\t" \
    "v_smfmac_f32_16x16x64_bf16 v[48:51], v[16:19], a[56:63], v1\n\t" \
    "v_smfmac_f32_16x16x64_bf16 v[52:55], v[16:19], a[112:119], v1\n\t" \
    "v_smfmac_f32_16x16x64_bf16 v[56:59], v[16:19], a[168:175], v1\n\t" \
    "v_smfmac_f32_16x16x64_bf16 v[60:63], v[16:19], a[224:231], v1\n\t" \
    "v_smfmac_f32_16x16x64_bf16 v[64:67], v[16:19], v[144:151], v1\n\t" \
    "v_smfmac_f32_16x16x64_bf16 v[68:71], v[16:19], v[200:207], v1\n\t" \
    "v_smfmac_f32_16x16x64_bf16 v[72:75], v[16:19], v[80:87], v1\n\t" \
    "v_smfmac_f32_16x16x64_bf16 v[76:79], v[16:19], v[88:95], v1\n\t" \
    "ds_read_b128 v[80:83], v5 offset:4096\n\t" \
    "ds_read_b128 v[84:87], v5 offset:5120\n\t" \
    "ds_read_b128 v[88:91], v5 offset:18432\n\t" \
    "ds_read_b128 v[92:95], v5 offset:19456\n\t" \
    "v_smfmac_f32_16x16x64_bf16 v[44:47], v[20:23], a[8:15], v1\n\t" \
    "v_smfmac_f32_16x16x64_bf16 v[48:51], v[20:23], a[64:71], v1\n\t" \
    "v_smfmac_f32_16x16x64_bf16 v[52:55], v[20:23], a[120:127], v1\n\t" \
    "v_smfmac_f32_16x16x64_bf16 v[56:59], v[20:23], a[176:183], v1\n\t" \
    "v_smfmac_f32_16x16x64_bf16 v[60:63], v[20:23], a[232:239], v1\n\t" \
    "v_smfmac_f32_16x16x64_bf16 v[64:67], v[20:23], v[152:159], v1\n\t" \
    "v_smfmac_f32_16x16x64_bf16 v[68:71], v[20:23], v[208:215], v1\n\t" \
    "v_smfmac_f32_16x16x64_bf16 v[72:75], v[20:23], v[96:103], v1\n\t" \
    "v_smfmac_f32_16x16x64_bf16 v[76:79], v[20:23], v[104:111], v1\n\t" \
    "ds_read_b128 v[96:99], v5 offset:6144\n\t" \
    "ds_read_b128 v[100:103], v5 offset:7168\n\t" \
    "ds_read_b128 v[104:107], v5 offset:20480\n\t" \
    "ds_read_b128 v[108:111], v5 offset:21504\n\t" \
    "v_smfmac_f32_16x16x64_bf16 v[44:47], v[24:27], a[16:23], v1\n\t" \
    "v_smfmac_f32_16x16x64_bf16 v[48:51], v[24:27], a[72:79], v1\n\t" \
    "v_smfmac_f32_16x16x64_bf16 v[52:55], v[24:27], a[128:135], v1\n\t" \
    "v_smfmac_f32_16x16x64_bf16 v[56:59], v[24:27], a[184:191], v1\n\t" \
    "v_smfmac_f32_16x16x64_bf16 v[60:63], v[24:27], a[240:247], v1\n\t" \
    "v_smfmac_f32_16x16x64_bf16 v[64:67], v[24:27], v[160:167], v1\n\t" \
    "v_smfmac_f32_16x16x64_bf16 v[68:71], v[24:27], v[216:223], v1\n\t" \
    "s_waitcnt lgkmcnt(4)\n\t" \
    "v_smfmac_f32_16x16x64_bf16 v[72:75], v[24:27], v[80:87], v1\n\t" \
    "v_smfmac_f32_16x16x64_bf16 v[76:79], v[24:27], v[88:95], v1\n\t" \
    "ds_read_b128 v[80:83], v5 offset:8192\n\t" \
    "ds_read_b128 v[84:87], v5 offset:9216\n\t" \
    "ds_read_b128 v[88:91], v5 offset:22528\n\t" \
    "ds_read_b128 v[92:95], v5 offset:23552\n\t" \
    "s_waitcnt lgkmcnt(15)\n\t" \
    "v_smfmac_f32_16x16x64_bf16 v[44:47], v[28:31], a[24:31], v1\n\t" \
    "v_smfmac_f32_16x16x64_bf16 v[48:51], v[28:31], a[80:87], v1\n\t" \
    "v_smfmac_f32_16x16x64_bf16 v[52:55], v[28:31], a[136:143], v1\n\t" \
    "v_smfmac_f32_16x16x64_bf16 v[56:59], v[28:31], a[192:199], v1\n\t" \
    "v_smfmac_f32_16x16x64_bf16 v[60:63], v[28:31], a[248:255], v1\n\t" \
    "v_smfmac_f32_16x16x64_bf16 v[64:67], v[28:31], v[168:175], v1\n\t" \
    "v_smfmac_f32_16x16x64_bf16 v[68:71], v[28:31], v[224:231], v1\n\t" \
    "s_waitcnt lgkmcnt(4)\n\t" \
    "v_smfmac_f32_16x16x64_bf16 v[72:75], v[28:31], v[96:103], v1\n\t" \
    "v_smfmac_f32_16x16x64_bf16 v[76:79], v[28:31], v[104:111], v1\n\t" \
    "ds_read_b128 v[96:99], v5 offset:10240\n\t" \
    "ds_read_b128 v[100:103], v5 offset:11264\n\t" \
    "ds_read_b128 v[104:107], v5 offset:24576\n\t" \
    "ds_read_b128 v[108:111], v5 offset:25600\n\t" \
    "s_waitcnt lgkmcnt(15)\n\t" \
    "v_smfmac_f32_16x16x64_bf16 v[44:47], v[32:35], a[32:39], v1\n\t" \
    "v_smfmac_f32_16x16x64_bf16 v[48:51], v[32:35], a[88:95], v1\n\t" \
    "v_smfmac_f32_16x16x64_bf16 v[52:55], v[32:35], a[144:151], v1\n\t" \
    "v_smfmac_f32_16x16x64_bf16 v[56:59], v[32:35], a[200:207], v1\n\t" \
    "v_smfmac_f32_16x16x64_bf16 v[60:63], v[32:35], v[120:127], v1\n\t" \
    "v_smfmac_f32_16x16x64_bf16 v[64:67], v[32:35], v[176:183], v1\n\t" \
    "v_smfmac_f32_16x16x64_bf16 v[68:71], v[32:35], v[232:239], v1\n\t" \
    "s_waitcnt lgkmcnt(4)\n\t" \
    "v_smfmac_f32_16x16x64_bf16 v[72:75], v[32:35], v[80:87], v1\n\t" \
    "v_smfmac_f32_16x16x64_bf16 v[76:79], v[32:35], v[88:95], v1\n\t" \
    "ds_read_b128 v[80:83], v5 offset:12288\n\t" \
    "ds_read_b128 v[84:87], v5 offset:13312\n\t" \
    "ds_read_b128 v[88:91], v5 offset:26624\n\t" \
    "ds_read_b128 v[92:95], v5 offset:27648\n\t" \
    "s_waitcnt lgkmcnt(15)\n\t" \
    "v_smfmac_f32_16x16x64_bf16 v[44:47], v[36:39], a[40:47], v1\n\t" \
    "v_smfmac_f32_16x16x64_bf16 v[48:51], v[36:39], a[96:103], v1\n\t" \
    "v_smfmac_f32_16x16x64_bf16 v[52:55], v[36:39], a[152:159], v1\n\t" \
    "v_smfmac_f32_16x16x64_bf16 v[56:59], v[36:39], a[208:215], v1\n\t" \
    "v_smfmac_f32_16x16x64_bf16 v[60:63], v[36:39], v[128:135], v1\n\t" \
    "v_smfmac_f32_16x16x64_bf16 v[64:67], v[36:39], v[184:191], v1\n\t" \
    "v_smfmac_f32_16x16x64_bf16 v[68:71], v[36:39], v[240:247], v1\n\t" \
    "s_waitcnt lgkmcnt(4)\n\t" \
    "v_smfmac_f32_16x16x64_bf16 v[72:75], v[36:39], v[96:103], v1\n\t" \
    "v_smfmac_f32_16x16x64_bf16 v[76:79], v[36:39], v[104:111], v1\n\t" \
    "s_waitcnt lgkmcnt(15)\n\t" \
    "v_smfmac_f32_16x16x64_bf16 v[44:47], v[40:43], a[48:55], v1\n\t" \
    "v_smfmac_f32_16x16x64_bf16 v[48:51], v[40:43], a[104:111], v1\n\t" \
    "v_smfmac_f32_16x16x64_bf16 v[52:55], v[40:43], a[160:167], v1\n\t" \
    "v_smfmac_f32_16x16x64_bf16 v[56:59], v[40:43], a[216:223], v1\n\t" \
    "v_smfmac_f32_16x16x64_bf16 v[60:63], v[40:43], v[136:143], v1\n\t" \
    "v_smfmac_f32_16x16x64_bf16 v[64:67], v[40:43], v[192:199], v1\n\t" \
    "v_smfmac_f32_16x16x64_bf16 v[68:71], v[40:43], v[248:255], v1\n\t" \
    "s_waitcnt lgkmcnt(0)\n\t" \
    "v_smfmac_f32_16x16x64_bf16 v[72:75], v[40:43], v[80:87], v1\n\t" \
    "v_smfmac_f32_16x16x64_bf16 v[76:79], v[40:43], v[88:95], v1\n\t" \
    "s_nop 7\n\t" \
    "v_add_f32 v44, v44, v45\n\t" \
    "v_add_f32 v46, v46, v47\n\t" \
    "v_add_f32 v48, v48, v49\n\t" \
    "v_add_f32 v50, v50, v51\n\t" \
    "v_add_f32 v52, v52, v53\n\t" \
    "v_add_f32 v54, v54, v55\n\t" \
    "v_add_f32 v56, v56, v57\n\t" \
    "v_add_f32 v58, v58, v59\n\t" \
    "v_add_f32 v60, v60, v61\n\t" \
    "v_add_f32 v62, v62, v63\n\t" \
    "v_add_f32 v64, v64, v65\n\t" \
    "v_add_f32 v66, v66, v67\n\t" \
    "v_add_f32 v68, v68, v69\n\t" \
    "v_add_f32 v70, v70, v71\n\t" \
    "v_add_f32 v72, v72, v73\n\t" \
    "v_add_f32 v74, v74, v75\n\t" \
    "v_add_f32 v76, v76, v77\n\t" \
    "v_add_f32 v78, v78, v79\n\t" \
    "v_cvt_pk_bf16_f32 v44, v44, v46\n\t" \
    "v_cvt_pk_bf16_f32 v48, v48, v50\n\t" \
    "v_cvt_pk_bf16_f32 v52, v52, v54\n\t" \
    "v_cvt_pk_bf16_f32 v56, v56, v58\n\t" \
    "v_cvt_pk_bf16_f32 v60, v60, v62\n\t" \
    "v_cvt_pk_bf16_f32 v64, v64, v66\n\t" \
    "v_cvt_pk_bf16_f32 v68, v68, v70\n\t" \
    "v_cvt_pk_bf16_f32 v72, v72, v74\n\t" \
    "v_cvt_pk_bf16_f32 v76, v76, v78\n\t" \
    "ds_write_b32 v3, v44 offset:2560\n\t" \
    "ds_write_b32 v3, v48 offset:516\n\t" \
    "ds_write_b32 v3, v52 offset:2564\n\t" \
    "ds_write_b32 v3, v56 offset:1024\n\t" \
    "ds_write_b32 v3, v60 offset:3072\n\t" \
    "ds_write_b32 v3, v64 offset:1028\n\t" \
    "ds_write_b32 v3, v68 offset:3076\n\t" \
    "ds_write_b32 v3, v72 offset:1536\n\t" \
    "ds_write_b32 v3, v76 offset:3584\n\t" \
    "s_waitcnt lgkmcnt(0)\n\t" \
    "s_barrier\n\t" \
    "ds_read_b128 v[16:19], v6 offset:0\n\t" \
    "ds_read_b128 v[20:23], v6 offset:1024\n\t" \
    "ds_read_b128 v[24:27], v6 offset:2048\n\t" \
    "s_waitcnt lgkmcnt(2)\n\t" \
    "buffer_store_dwordx4 v[16:19], v0, s[8:11], s33 offen sc1\n\t" \
    "s_waitcnt lgkmcnt(1)\n\t" \
    "buffer_store_dwordx4 v[20:23], v0, s[8:11], s33 offen offset:1024 sc1\n\t" \
    "s_waitcnt lgkmcnt(0)\n\t" \
    "buffer_store_dwordx4 v[24:27], v0, s[8:11], s33 offen offset:2048 sc1\n\t" \
    "s_add_u32 s28, s28, 1\n\t" \
    "s_cmp_eq_u32 s28, 64\n\t" \
    "s_cbranch_scc1 LZ1_end_%=\n\t" \
    "s_add_u32 s33, s33, 114688\n\t" \
    "s_add_u32 s34, s34, 114688\n\t" \
    "s_branch LZ1_loop_%=\n\t" \
    "LZ1_fail_%=:\n\t" \
    "s_add_u32 s31, s31, 1\n\t" \
    "s_cmp_gt_u32 s31, 131072\n\t" \
    "s_cbranch_scc1 LZ1_end_%=\n\t" \
    "s_branch LZ1_retry_%=\n\t" \
    "LZ1_end_%=:\n\t" \
    "s_waitcnt vmcnt(0)\n\t" \
    "s_barrier\n\t" \
    "v_mov_b32 v10, s28\n\t" \
    "s_mov_b64 exec, 1\n\t" \
    "buffer_store_dword v10, off, s[8:11], s17 sc1\n\t" \
    "s_mov_b64 exec, -1\n\t" \
    "s_endpgm\n\t"
#define ASM_CHAIN_Z2 \
    "s_nop 7\n\t" \
    "s_mov_b32 s42, s13\n\t" \
    "buffer_load_dwordx4 a[0:3], v0, s[8:11], s42 offen\n\t" \
    "buffer_load_dwordx4 a[4:7], v0, s[8:11], s42 offen offset:1024\n\t" \
    "s_add_u32 s42, s42, 2048\n\t" \
    "buffer_load_dwordx4 a[8:11], v0, s[8:11], s42 offen\n\t" \
    "buffer_load_dwordx4 a[12:15], v0, s[8:11], s42 offen offset:1024\n\t" \
    "s_add_u32 s42, s42, 2048\n\t" \
    "buffer_load_dwordx4 a[16:19], v0, s[8:11], s42 offen\n\t" \
    "buffer_load_dwordx4 a[20:23], v0, s[8:11], s42 offen offset:1024\n\t" \
    "s_add_u32 s42, s42, 2048\n\t" \
    "buffer_load_dwordx4 a[24:27], v0, s[8:11], s42 offen\n\t" \
    "buffer_load_dwordx4 a[28:31], v0, s[8:11], s42 offen offset:1024\n\t" \
    "s_add_u32 s42, s42, 2048\n\t" \
    "buffer_load_dwordx4 a[32:35], v0, s[8:11], s42 offen\n\t" \
    "buffer_load_dwordx4 a[36:39], v0, s[8:11], s42 offen offset:1024\n\t" \
    "s_add_u32 s42, s42, 2048\n\t" \
    "buffer_load_dwordx4 a[40:43], v0, s[8:11], s42 offen\n\t" \
    "buffer_load_dwordx4 a[44:47], v0, s[8:11], s42 offen offset:1024\n\t" \
    "s_add_u32 s42, s42, 2048\n\t" \
    "buffer_load_dwordx4 a[48:51], v0, s[8:11], s42 offen\n\t" \
    "buffer_load_dwordx4 a[52:55], v0, s[8:11], s42 offen offset:1024\n\t" \
    "s_add_u32 s42, s42, 2048\n\t" \
    "buffer_load_dwordx4 a[56:59], v0, s[8:11], s42 offen\n\t" \
    "buffer_load_dwordx4 a[60:63], v0, s[8:11], s42 offen offset:1024\n\t" \
    "s_add_u32 s42, s42, 2048\n\t" \
    "buffer_load_dwordx4 a[64:67], v0, s[8:11], s42 offen\n\t" \
    "buffer_load_dwordx4 a[68:71], v0, s[8:11], s42 offen offset:1024\n\t" \
    "s_add_u32 s42, s42, 2048\n\t" \
    "buffer_load_dwordx4 a[72:75], v0, s[8:11], s42 offen\n\t" \
    "buffer_load_dwordx4 a[76:79], v0, s[8:11], s42 offen offset:1024\n\t" \
    "s_add_u32 s42, s42, 2048\n\t" \
    "buffer_load_dwordx4 a[80:83], v0, s[8:11], s42 offen\n\t" \
    "buffer_load_dwordx4 a[84:87], v0, s[8:11], s42 offen offset:1024\n\t" \
    "s_add_u32 s42, s42, 2048\n\t" \
    "buffer_load_dwordx4 a[88:91], v0, s[8:11], s42 offen\n\t" \
    "buffer_load_dwordx4 a[92:95], v0, s[8:11], s42 offen offset:1024\n\t" \
    "s_add_u32 s42, s42, 2048\n\t" \
    "buffer_load_dwordx4 a[96:99], v0, s[8:11], s42 offen\n\t" \
    "buffer_load_dwordx4 a[100:103], v0, s[8:11], s42 offen offset:1024\n\t" \
    "s_add_u32 s42, s42, 2048\n\t" \
    "buffer_load_dwordx4 a[104:107], v0, s[8:11], s42 offen\n\t" \
    "buffer_load_dwordx4 a[108:111], v0, s[8:11], s42 offen offset:1024\n\t" \
    "s_add_u32 s42, s42, 2048\n\t" \
    "buffer_load_dwordx4 a[112:115], v0, s[8:11], s42 offen\n\t" \
    "buffer_load_dwordx4 a[116:119], v0, s[8:11], s42 offen offset:1024\n\t" \
    "s_add_u32 s42, s42, 2048\n\t" \
    "buffer_load_dwordx4 a[120:123], v0, s[8:11], s42 offen\n\t" \
    "buffer_load_dwordx4 a[124:127], v0, s[8:11], s42 offen offset:1024\n\t" \
    "s_add_u32 s42, s42, 2048\n\t" \
    "buffer_load_dwordx4 a[128:131], v0, s[8:11], s42 offen\n\t" \
    "buffer_load_dwordx4 a[132:135], v0, s[8:11], s42 offen offset:1024\n\t" \
    "s_add_u32 s42, s42, 2048\n\t" \
    "buffer_load_dwordx4 a[136:139], v0, s[8:11], s42 offen\n\t" \
    "buffer_load_dwordx4 a[140:143], v0, s[8:11], s42 offen offset:1024\n\t" \
    "s_add_u32 s42, s42, 2048\n\t" \
    "buffer_load_dwordx4 a[144:147], v0, s[8:11], s42 offen\n\t" \
    "buffer_load_dwordx4 a[148:151], v0, s[8:11], s42 offen offset:1024\n\t" \
    "s_add_u32 s42, s42, 2048\n\t" \
    "buffer_load_dwordx4 a[152:155], v0, s[8:11], s42 offen\n\t" \
    "buffer_load_dwordx4 a[156:159], v0, s[8:11], s42 offen offset:1024\n\t" \
    "s_add_u32 s42, s42, 2048\n\t" \
    "buffer_load_dwordx4 a[160:163], v0, s[8:11], s42 offen\n\t" \
    "buffer_load_dwordx4 a[164:167], v0, s[8:11], s42 offen offset:1024\n\t" \
    "s_add_u32 s42, s42, 2048\n\t" \
    "buffer_load_dwordx4 a[168:171], v0, s[8:11], s42 offen\n\t" \
    "buffer_load_dwordx4 a[172:175], v0, s[8:11], s42 offen offset:1024\n\t" \
    "s_add_u32 s42, s42, 2048\n\t" \
    "buffer_load_dwordx4 a[176:179], v0, s[8:11], s42 offen\n\t" \
    "buffer_load_dwordx4 a[180:183], v0, s[8:11], s42 offen offset:1024\n\t" \
    "s_add_u32 s42, s42, 2048\n\t" \
    "buffer_load_dwordx4 a[184:187], v0, s[8:11], s42 offen\n\t" \
    "buffer_load_dwordx4 a[188:191], v0, s[8:11], s42 offen offset:1024\n\t" \
    "s_add_u32 s42, s42, 2048\n\t" \
    "buffer_load_dwordx4 a[192:195], v0, s[8:11], s42 offen\n\t" \
    "buffer_load_dwordx4 a[196:199], v0, s[8:11], s42 offen offset:1024\n\t" \
    "s_add_u32 s42, s42, 2048\n\t" \
    "buffer_load_dwordx4 a[200:203], v0, s[8:11], s42 offen\n\t" \
    "buffer_load_dwordx4 a[204:207], v0, s[8:11], s42 offen offset:1024\n\t" \
    "s_add_u32 s42, s42, 2048\n\t" \
    "buffer_load_dwordx4 a[208:211], v0, s[8:11], s42 offen\n\t" \
    "buffer_load_dwordx4 a[212:215], v0, s[8:11], s42 offen offset:1024\n\t" \
    "s_add_u32 s42, s42, 2048\n\t" \
    "s_waitcnt vmcnt(52)\n\t" \
    "buffer_load_dwordx4 a[216:219], v0, s[8:11], s42 offen\n\t" \
    "buffer_load_dwordx4 a[220:223], v0, s[8:11], s42 offen offset:1024\n\t" \
    "s_add_u32 s42, s42, 2048\n\t" \
    "s_waitcnt vmcnt(52)\n\t" \
    "buffer_load_dwordx4 a[224:227], v0, s[8:11], s42 offen\n\t" \
    "buffer_load_dwordx4 a[228:231], v0, s[8:11], s42 offen offset:1024\n\t" \
    "s_add_u32 s42, s42, 2048\n\t" \
    "s_waitcnt vmcnt(52)\n\t" \
    "buffer_load_dwordx4 a[232:235], v0, s[8:11], s42 offen\n\t" \
    "buffer_load_dwordx4 a[236:239], v0, s[8:11], s42 offen offset:1024\n\t" \
    "s_add_u32 s42, s42, 2048\n\t" \
    "s_waitcnt vmcnt(52)\n\t" \
    "buffer_load_dwordx4 a[240:243], v0, s[8:11], s42 offen\n\t" \
    "buffer_load_dwordx4 a[244:247], v0, s[8:11], s42 offen offset:1024\n\t" \
    "s_add_u32 s42, s42, 2048\n\t" \
    "s_waitcnt vmcnt(52)\n\t" \
    "buffer_load_dwordx4 a[248:251], v0, s[8:11], s42 offen\n\t" \
    "buffer_load_dwordx4 a[252:255], v0, s[8:11], s42 offen offset:1024\n\t" \
    "s_add_u32 s42, s42, 2048\n\t" \
    "s_waitcnt vmcnt(52)\n\t" \
    "buffer_load_dwordx4 v[120:123], v0, s[8:11], s42 offen\n\t" \
    "buffer_load_dwordx4 v[124:127], v0, s[8:11], s42 offen offset:1024\n\t" \
    "s_add_u32 s42, s42, 2048\n\t" \
    "s_waitcnt vmcnt(52)\n\t" \
    "buffer_load_dwordx4 v[128:131], v0, s[8:11], s42 offen\n\t" \
    "buffer_load_dwordx4 v[132:135], v0, s[8:11], s42 offen offset:1024\n\t" \
    "s_add_u32 s42, s42, 2048\n\t" \
    "s_waitcnt vmcnt(52)\n\t" \
    "buffer_load_dwordx4 v[136:139], v0, s[8:11], s42 offen\n\t" \
    "buffer_load_dwordx4 v[140:143], v0, s[8:11], s42 offen offset:1024\n\t" \
    "s_add_u32 s42, s42, 2048\n\t" \
    "s_waitcnt vmcnt(52)\n\t" \
    "buffer_load_dwordx4 v[144:147], v0, s[8:11], s42 offen\n\t" \
    "buffer_load_dwordx4 v[148:151], v0, s[8:11], s42 offen offset:1024\n\t" \
    "s_add_u32 s42, s42, 2048\n\t" \
    "s_waitcnt vmcnt(52)\n\t" \
    "buffer_load_dwordx4 v[152:155], v0, s[8:11], s42 offen\n\t" \
    "buffer_load_dwordx4 v[156:159], v0, s[8:11], s42 offen offset:1024\n\t" \
    "s_add_u32 s42, s42, 2048\n\t" \
    "s_waitcnt vmcnt(52)\n\t" \
    "buffer_load_dwordx4 v[160:163], v0, s[8:11], s42 offen\n\t" \
    "buffer_load_dwordx4 v[164:167], v0, s[8:11], s42 offen offset:1024\n\t" \
    "s_add_u32 s42, s42, 2048\n\t" \
    "s_waitcnt vmcnt(52)\n\t" \
    "buffer_load_dwordx4 v[168:171], v0, s[8:11], s42 offen\n\t" \
    "buffer_load_dwordx4 v[172:175], v0, s[8:11], s42 offen offset:1024\n\t" \
    "s_add_u32 s42, s42, 2048\n\t" \
    "s_waitcnt vmcnt(52)\n\t" \
    "buffer_load_dwordx4 v[176:179], v0, s[8:11], s42 offen\n\t" \
    "buffer_load_dwordx4 v[180:183], v0, s[8:11], s42 offen offset:1024\n\t" \
    "s_add_u32 s42, s42, 2048\n\t" \
    "s_waitcnt vmcnt(52)\n\t" \
    "buffer_load_dwordx4 v[184:187], v0, s[8:11], s42 offen\n\t" \
    "buffer_load_dwordx4 v[188:191], v0, s[8:11], s42 offen offset:1024\n\t" \
    "s_add_u32 s42, s42, 2048\n\t" \
    "s_waitcnt vmcnt(52)\n\t" \
    "buffer_load_dwordx4 v[192:195], v0, s[8:11], s42 offen\n\t" \
    "buffer_load_dwordx4 v[196:199], v0, s[8:11], s42 offen offset:1024\n\t" \
    "s_add_u32 s42, s42, 2048\n\t" \
    "s_waitcnt vmcnt(52)\n\t" \
    "buffer_load_dwordx4 v[200:203], v0, s[8:11], s42 offen\n\t" \
    "buffer_load_dwordx4 v[204:207], v0, s[8:11], s42 offen offset:1024\n\t" \
    "s_add_u32 s42, s42, 2048\n\t" \
    "s_waitcnt vmcnt(52)\n\t" \
    "buffer_load_dwordx4 v[208:211], v0, s[8:11], s42 offen\n\t" \
    "buffer_load_dwordx4 v[212:215], v0, s[8:11], s42 offen offset:1024\n\t" \
    "s_add_u32 s42, s42, 2048\n\t" \
    "s_waitcnt vmcnt(52)\n\t" \
    "buffer_load_dwordx4 v[216:219], v0, s[8:11], s42 offen\n\t" \
    "buffer_load_dwordx4 v[220:223], v0, s[8:11], s42 offen offset:1024\n\t" \
    "s_add_u32 s42, s42, 2048\n\t" \
    "s_waitcnt vmcnt(52)\n\t" \
    "buffer_load_dwordx4 v[224:227], v0, s[8:11], s42 offen\n\t" \
    "buffer_load_dwordx4 v[228:231], v0, s[8:11], s42 offen offset:1024\n\t" \
    "s_add_u32 s42, s42, 2048\n\t" \
    "s_waitcnt vmcnt(52)\n\t" \
    "buffer_load_dwordx4 v[232:235], v0, s[8:11], s42 offen\n\t" \
    "buffer_load_dwordx4 v[236:239], v0, s[8:11], s42 offen offset:1024\n\t" \
    "s_add_u32 s42, s42, 2048\n\t" \
    "s_waitcnt vmcnt(52)\n\t" \
    "buffer_load_dwordx4 v[240:243], v0, s[8:11], s42 offen\n\t" \
    "buffer_load_dwordx4 v[244:247], v0, s[8:11], s42 offen offset:1024\n\t" \
    "s_add_u32 s42, s42, 2048\n\t" \
    "s_waitcnt vmcnt(52)\n\t" \
    "buffer_load_dwordx4 v[248:251], v0, s[8:11], s42 offen\n\t" \
    "buffer_load_dwordx4 v[252:255], v0, s[8:11], s42 offen offset:1024\n\t" \
    "s_add_u32 s42, s42, 2048\n\t" \
    "s_waitcnt vmcnt(52)\n\t" \
    "s_waitcnt vmcnt(0)\n\t" \
    "s_mov_b32 s28, 0\n\t" \
    "s_mov_b32 s31, 0\n\t" \
    "s_mov_b32 s30, 0\n\t" \
    "s_mov_b32 s33, s15\n\t" \
    "s_add_u32 s34, s15, 4096\n\t" \
    "LZ2_loop_%=:\n\t" \
    "s_lshr_b32 s42, s30, 5\n\t" \
    "s_lshl_b32 s42, s42, 2\n\t" \
    "s_and_b32 s45, s30, 1\n\t" \
    "s_lshl_b32 s45, s45, 1\n\t" \
    "s_add_u32 s42, s42, s45\n\t" \
    "s_lshr_b32 s42, s12, s42\n\t" \
    "s_and_b32 s42, s42, 3\n\t" \
    "s_lshl_b32 s42, s42, 13\n\t" \
    "s_lshr_b32 s45, s30, 1\n\t" \
    "s_lshl_b32 s45, s45, 15\n\t" \
    "s_add_u32 s35, s42, s45\n\t" \
    "s_add_u32 s30, s30, 3\n\t" \
    "s_add_u32 s44, s35, 3072\n\t" \
    "s_barrier\n\t" \
    "ds_read_b128 v[80:83], v5 offset:6144\n\t" \
    "ds_read_b128 v[84:87], v5 offset:7168\n\t" \
    "ds_read_b128 v[88:91], v5 offset:20480\n\t" \
    "ds_read_b128 v[92:95], v5 offset:21504\n\t" \
    "ds_read_b128 v[96:99], v5 offset:8192\n\t" \
    "ds_read_b128 v[100:103], v5 offset:9216\n\t" \
    "ds_read_b128 v[104:107], v5 offset:22528\n\t" \
    "ds_read_b128 v[108:111], v5 offset:23552\n\t" \
    "v_mov_b64 v[44:45], 0\n\t" \
    "v_mov_b64 v[46:47], 0\n\t" \
    "v_mov_b64 v[48:49], 0\n\t" \
    "v_mov_b64 v[50:51], 0\n\t" \
    "v_mov_b64 v[52:53], 0\n\t" \
    "v_mov_b64 v[54:55], 0\n\t" \
    "v_mov_b64 v[56:57], 0\n\t" \
    "v_mov_b64 v[58:59], 0\n\t" \
    "v_mov_b64 v[60:61], 0\n\t" \
    "v_mov_b64 v[62:63], 0\n\t" \
    "v_mov_b64 v[64:65], 0\n\t" \
    "v_mov_b64 v[66:67], 0\n\t" \
    "v_mov_b64 v[68:69], 0\n\t" \
    "v_mov_b64 v[70:71], 0\n\t" \
    "v_mov_b64 v[72:73], 0\n\t" \
    "v_mov_b64 v[74:75], 0\n\t" \
    "v_mov_b64 v[76:77], 0\n\t" \
    "v_mov_b64 v[78:79], 0\n\t" \
    "s_mov_b32 s42, s55\n\t" \
    "s_max_i32 s42, s42, 0\n\t" \
    "s_min_i32 s42, s42, 384\n\t" \
    "s_lshl_b32 s42, s42, 2\n\t" \
    "s_sub_u32 s42, 1548, s42\n\t" \
    "s_getpc_b64 s[40:41]\n\t" \
    "s_add_u32 s40, s40, s42\n\t" \
    "s_addc_u32 s41, s41, 0\n\t" \
    "s_setpc_b64 s[40:41]\n\t" \
    ".rept 384\n\t" \
    "s_nop 1\n\t" \
    ".endr\n\t" \
    "LZ2_retry_%=:\n\t" \
    "buffer_load_dwordx4 v[28:31], v0, s[8:11], s44 offen sc1\n\t" \
    "buffer_load_dwordx4 v[32:35], v0, s[8:11], s44 offen offset:1024 sc1\n\t" \
    "s_waitcnt vmcnt(1)\n\t" \
    "ds_write_b128 v6, v[28:31] offset:11264\n\t" \
    "v_max3_u32 v10, v28, v29, v30\n\t" \
    "v_max_u32 v10, v10, v31\n\t" \
    "s_waitcnt vmcnt(0)\n\t" \
    "ds_write_b128 v6, v[32:35] offset:12288\n\t" \
    "v_max3_u32 v10, v32, v33, v10\n\t" \
    "v_max3_u32 v10, v34, v35, v10\n\t" \
    "v_cmp_eq_u32 vcc, -1, v10\n\t" \
    "s_nop 1\n\t" \
    "s_cbranch_vccnz LZ2_fail_%=\n\t" \
    "s_waitcnt lgkmcnt(0)\n\t" \
    "s_barrier\n\t" \
    "ds_read_b128 v[16:19], v6 offset:8192\n\t" \
    "ds_read_b128 v[20:23], v6 offset:9216\n\t" \
    "ds_read_b128 v[24:27], v6 offset:10240\n\t" \
    "ds_read_b128 v[36:39], v6 offset:13312\n\t" \
    "ds_read_b128 v[40:43], v6 offset:14336\n\t" \
    "v_smfmac_f32_16x16x64_bf16 v[44:47], v[28:31], a[24:31], v1\n\t" \
    "v_smfmac_f32_16x16x64_bf16 v[48:51], v[28:31], a[80:87], v1\n\t" \
    "v_smfmac_f32_16x16x64_bf16 v[52:55], v[28:31], a[136:143], v1\n\t" \
    "v_smfmac_f32_16x16x64_bf16 v[56:59], v[28:31], a[192:199], v1\n\t" \
    "v_smfmac_f32_16x16x64_bf16 v[60:63], v[28:31], a[248:255], v1\n\t" \
    "v_smfmac_f32_16x16x64_bf16 v[64:67], v[28:31], v[168:175], v1\n\t" \
    "v_smfmac_f32_16x16x64_bf16 v[68:71], v[28:31], v[224:231], v1\n\t" \
    "v_smfmac_f32_16x16x64_bf16 v[72:75], v[28:31], v[80:87], v1\n\t" \
    "v_smfmac_f32_16x16x64_bf16 v[76:79], v[28:31], v[88:95], v1\n\t" \
    "ds_read_b128 v[80:83], v5 offset:0\n\t" \
    "ds_read_b128 v[84:87], v5 offset:1024\n\t" \
    "ds_read_b128 v[88:91], v5 offset:14336\n\t" \
    "ds_read_b128 v[92:95], v5 offset:15360\n\t" \
    "v_smfmac_f32_16x16x64_bf16 v[44:47], v[32:35], a[32:39], v1\n\t" \
    "v_smfmac_f32_16x16x64_bf16 v[48:51], v[32:35], a[88:95], v1\n\t" \
    "v_smfmac_f32_16x16x64_bf16 v[52:55], v[32:35], a[144:151], v1\n\t" \
    "v_smfmac_f32_16x16x64_bf16 v[56:59], v[32:35], a[200:207], v1\n\t" \
    "v_smfmac_f32_16x16x64_bf16 v[60:63], v[32:35], v[120:127], v1\n\t" \
    "v_smfmac_f32_16x16x64_bf16 v[64:67], v[32:35], v[176:183], v1\n\t" \
    "v_smfmac_f32_16x16x64_bf16 v[68:71], v[32:35], v[232:239], v1\n\t" \
    "v_smfmac_f32_16x16x64_bf16 v[72:75], v[32:35], v[96:103], v1\n\t" \
    "v_smfmac_f32_16x16x64_bf16 v[76:79], v[32:35], v[104:111], v1\n\t" \
    "ds_read_b128 v[96:99], v5 offset:2048\n\t" \
    "ds_read_b128 v[100:103], v5 offset:3072\n\t" \
    "ds_read_b128 v[104:107], v5 offset:16384\n\t" \
    "ds_read_b128 v[108:111], v5 offset:17408\n\t" \
    "s_waitcnt lgkmcnt(12)\n\t" \
    "v_smfmac_f32_16x16x64_bf16 v[44:47], v[16:19], a[0:7], v1\n\t" \
    "v_smfmac_f32_16x16x64_bf16 v[48:51], v[16:19], a[56:63], v1\n\t" \
    "v_smfmac_f32_16x16x64_bf16 v[52:55], v[16:19], a[112:119], v1\n\t" \
    "v_smfmac_f32_16x16x64_bf16 v[56:59], v[16:19], a[168:175], v1\n\t" \
    "v_smfmac_f32_16x16x64_bf16 v[60:63], v[16:19], a[224:231], v1\n\t" \
    "v_smfmac_f32_16x16x64_bf16 v[64:67], v[16:19], v[144:151], v1\n\t" \
    "v_smfmac_f32_16x16x64_bf16 v[68:71], v[16:19], v[200:207], v1\n\t" \
    "s_waitcnt lgkmcnt(4)\n\t" \
    "v_smfmac_f32_16x16x64_bf16 v[72:75], v[16:19], v[80:87], v1\n\t" \
    "v_smfmac_f32_16x16x64_bf16 v[76:79], v[16:19], v[88:95], v1\n\t" \
    "ds_read_b128 v[80:83], v5 offset:4096\n\t" \
    "ds_read_b128 v[84:87], v5 offset:5120\n\t" \
    "ds_read_b128 v[88:91], v5 offset:18432\n\t" \
    "ds_read_b128 v[92:95], v5 offset:19456\n\t" \
    "s_waitcnt lgkmcnt(15)\n\t" \
    "v_smfmac_f32_16x16x64_bf16 v[44:47], v[20:23], a[8:15], v1\n\t" \
    "v_smfmac_f32_16x16x64_bf16 v[48:51], v[20:23], a[64:71], v1\n\t" \
    "v_smfmac_f32_16x16x64_bf16 v[52:55], v[20:23], a[120:127], v1\n\t" \
    "v_smfmac_f32_16x16x64_bf16 v[56:59], v[20:23], a[176:183], v1\n\t" \
    "v_smfmac_f32_16x16x64_bf16 v[60:63], v[20:23], a[232:239], v1\n\t" \
    "v_smfmac_f32_16x16x64_bf16 v[64:67], v[20:23], v[152:159], v1\n\t" \
    "v_smfmac_f32_16x16x64_bf16 v[68:71], v[20:23], v[208:215], v1\n\t" \
    "s_waitcnt lgkmcnt(4)\n\t" \
    "v_smfmac_f32_16x16x64_bf16 v[72:75], v[20:23], v[96:103], v1\n\t" \
    "v_smfmac_f32_16x16x64_bf16 v[76:79], v[20:23], v[104:111], v1\n\t" \
    "ds_read_b128 v[96:99], v5 offset:10240\n\t" \
    "ds_read_b128 v[100:103], v5 offset:11264\n\t" \
    "ds_read_b128 v[104:107], v5 offset:24576\n\t" \
    "ds_read_b128 v[108:111], v5 offset:25600\n\t" \
    "s_waitcnt lgkmcnt(15)\n\t" \
    "v_smfmac_f32_16x16x64_bf16 v[44:47], v[24:27], a[16:23], v1\n\t" \
    "v_smfmac_f32_16x16x64_bf16 v[48:51], v[24:27], a[72:79], v1\n\t" \
    "v_smfmac_f32_16x16x64_bf16 v[52:55], v[24:27], a[128:135], v1\n\t" \
    "v_smfmac_f32_16x16x64_bf16 v[56:59], v[24:27], a[184:191], v1\n\t" \
    "v_smfmac_f32_16x16x64_bf16 v[60:63], v[24:27], a[240:247], v1\n\t" \
    "v_smfmac_f32_16x16x64_bf16 v[64:67], v[24:27], v[160:167], v1\n\t" \
    "v_smfmac_f32_16x16x64_bf16 v[68:71], v[24:27], v[216:223], v1\n\t" \
    "s_waitcnt lgkmcnt(4)\n\t" \
    "v_smfmac_f32_16x16x64_bf16 v[72:75], v[24:27], v[80:87], v1\n\t" \
    "v_smfmac_f32_16x16x64_bf16 v[76:79], v[24:27], v[88:95], v1\n\t" \
    "ds_read_b128 v[80:83], v5 offset:12288\n\t" \
    "ds_read_b128 v[84:87], v5 offset:13312\n\t" \
    "ds_read_b128 v[88:91], v5 offset:26624\n\t" \
    "ds_read_b128 v[92:95], v5 offset:27648\n\t" \
    "s_waitcnt lgkmcnt(15)\n\t" \
    "v_smfmac_f32_16x16x64_bf16 v[44:47], v[36:39], a[40:47], v1\n\t" \
    "v_smfmac_f32_16x16x64_bf16 v[48:51], v[36:39], a[96:103], v1\n\t" \
    "v_smfmac_f32_16x16x64_bf16 v[52:55], v[36:39], a[152:159], v1\n\t" \
    "v_smfmac_f32_16x16x64_bf16 v[56:59], v[36:39], a[208:215], v1\n\t" \
    "v_smfmac_f32_16x16x64_bf16 v[60:63], v[36:39], v[128:135], v1\n\t" \
    "v_smfmac_f32_16x16x64_bf16 v[64:67], v[36:39], v[184:191], v1\n\t" \
    "v_smfmac_f32_16x16x64_bf16 v[68:71], v[36:39], v[240:247], v1\n\t" \
    "s_waitcnt lgkmcnt(4)\n\t" \
    "v_smfmac_f32_16x16x64_bf16 v[72:75], v[36:39], v[96:103], v1\n\t" \
    "v_smfmac_f32_16x16x64_bf16 v[76:79], v[36:39], v[104:111], v1\n\t" \
    "s_waitcnt lgkmcnt(15)\n\t" \
    "v_smfmac_f32_16x16x64_bf16 v[44:47], v[40:43], a[48:55], v1\n\t" \
    "v_smfmac_f32_16x16x64_bf16 v[48:51], v[40:43], a[104:111], v1\n\t" \
    "v_smfmac_f32_16x16x64_bf16 v[52:55], v[40:43], a[160:167], v1\n\t" \
    "v_smfmac_f32_16x16x64_bf16 v[56:59], v[40:43], a[216:223], v1\n\t" \
    "v_smfmac_f32_16x16x64_bf16 v[60:63], v[40:43], v[136:143], v1\n\t" \
    "v_smfmac_f32_16x16x64_bf16 v[64:67], v[40:43], v[192:199], v1\n\t" \
    "v_smfmac_f32_16x16x64_bf16 v[68:71], v[40:43], v[248:255], v1\n\t" \
    "s_waitcnt lgkmcnt(0)\n\t" \
    "v_smfmac_f32_16x16x64_bf16 v[72:75], v[40:43], v[80:87], v1\n\t" \
    "v_smfmac_f32_16x16x64_bf16 v[76:79], v[40:43], v[88:95], v1\n\t" \
    "s_nop 7\n\t" \
    "v_add_f32 v44, v44, v45\n\t" \
    "v_add_f32 v46, v46, v47\n\t" \
    "v_add_f32 v48, v48, v49\n\t" \
    "v_add_f32 v50, v50, v51\n\t" \
    "v_add_f32 v52, v52, v53\n\t" \
    "v_add_f32 v54, v54, v55\n\t" \
    "v_add_f32 v56, v56, v57\n\t" \
    "v_add_f32 v58, v58, v59\n\t" \
    "v_add_f32 v60, v60, v61\n\t" \
    "v_add_f32 v62, v62, v63\n\t" \
    "v_add_f32 v64, v64, v65\n\t" \
    "v_add_f32 v66, v66, v67\n\t" \
    "v_add_f32 v68, v68, v69\n\t" \
    "v_add_f32 v70, v70, v71\n\t" \
    "v_add_f32 v72, v72, v73\n\t" \
    "v_add_f32 v74, v74, v75\n\t" \
    "v_add_f32 v76, v76, v77\n\t" \
    "v_add_f32 v78, v78, v79\n\t" \
    "v_cvt_pk_bf16_f32 v44, v44, v46\n\t" \
    "v_cvt_pk_bf16_f32 v48, v48, v50\n\t" \
    "v_cvt_pk_bf16_f32 v52, v52, v54\n\t" \
    "v_cvt_pk_bf16_f32 v56, v56, v58\n\t" \
    "v_cvt_pk_bf16_f32 v60, v60, v62\n\t" \
    "v_cvt_pk_bf16_f32 v64, v64, v66\n\t" \
    "v_cvt_pk_bf16_f32 v68, v68, v70\n\t" \
    "v_cvt_pk_bf16_f32 v72, v72, v74\n\t" \
    "v_cvt_pk_bf16_f32 v76, v76, v78\n\t" \
    "ds_write_b32 v3, v44 offset:1540\n\t" \
    "ds_write_b32 v3, v48 offset:3588\n\t" \
    "ds_write_b32 v3, v52 offset:4096\n\t" \
    "ds_write_b32 v3, v56 offset:6144\n\t" \
    "ds_write_b32 v3, v60 offset:4100\n\t" \
    "ds_write_b32 v3, v64 offset:6148\n\t" \
    "ds_write_b32 v3, v68 offset:4608\n\t" \
    "ds_write_b32 v3, v72 offset:6656\n\t" \
    "ds_write_b32 v3, v76 offset:4612\n\t" \
    "s_waitcnt lgkmcnt(0)\n\t" \
    "s_barrier\n\t" \
    "ds_read_b128 v[16:19], v6 offset:3072\n\t" \
    "ds_read_b128 v[20:23], v6 offset:4096\n\t" \
    "ds_read_b128 v[24:27], v6 offset:5120\n\t" \
    "s_waitcnt lgkmcnt(2)\n\t" \
    "buffer_store_dwordx4 v[16:19], v0, s[8:11], s33 offen offset:3072 sc1\n\t" \
    "s_waitcnt lgkmcnt(1)\n\t" \
    "buffer_store_dwordx4 v[20:23], v0, s[8:11], s34 offen sc1\n\t" \
    "s_waitcnt lgkmcnt(0)\n\t" \
    "buffer_store_dwordx4 v[24:27], v0, s[8:11], s34 offen offset:1024 sc1\n\t" \
    "s_add_u32 s28, s28, 1\n\t" \
    "s_cmp_eq_u32 s28, 64\n\t" \
    "s_cbranch_scc1 LZ2_end_%=\n\t" \
    "s_add_u32 s33, s33, 114688\n\t" \
    "s_add_u32 s34, s34, 114688\n\t" \
    "s_branch LZ2_loop_%=\n\t" \
    "LZ2_fail_%=:\n\t" \
    "s_add_u32 s31, s31, 1\n\t" \
    "s_cmp_gt_u32 s31, 131072\n\t" \
    "s_cbranch_scc1 LZ2_end_%=\n\t" \
    "s_branch LZ2_retry_%=\n\t" \
    "LZ2_end_%=:\n\t" \
    "s_waitcnt vmcnt(0)\n\t" \
    "s_barrier\n\t" \
    "s_endpgm\n\t"
#define ASM_CHAIN_Z3 \
    "s_nop 7\n\t" \
    "s_mov_b32 s42, s13\n\t" \
    "buffer_load_dwordx4 a[0:3], v0, s[8:11], s42 offen\n\t" \
    "buffer_load_dwordx4 a[4:7], v0, s[8:11], s42 offen offset:1024\n\t" \
    "s_add_u32 s42, s42, 2048\n\t" \
    "buffer_load_dwordx4 a[8:11], v0, s[8:11], s42 offen\n\t" \
    "buffer_load_dwordx4 a[12:15], v0, s[8:11], s42 offen offset:1024\n\t" \
    "s_add_u32 s42, s42, 2048\n\t" \
    "buffer_load_dwordx4 a[16:19], v0, s[8:11], s42 offen\n\t" \
    "buffer_load_dwordx4 a[20:23], v0, s[8:11], s42 offen offset:1024\n\t" \
    "s_add_u32 s42, s42, 2048\n\t" \
    "buffer_load_dwordx4 a[24:27], v0, s[8:11], s42 offen\n\t" \
    "buffer_load_dwordx4 a[28:31], v0, s[8:11], s42 offen offset:1024\n\t" \
    "s_add_u32 s42, s42, 2048\n\t" \
    "buffer_load_dwordx4 a[32:35], v0, s[8:11], s42 offen\n\t" \
    "buffer_load_dwordx4 a[36:39], v0, s[8:11], s42 offen offset:1024\n\t" \
    "s_add_u32 s42, s42, 2048\n\t" \
    "buffer_load_dwordx4 a[40:43], v0, s[8:11], s42 offen\n\t" \
    "buffer_load_dwordx4 a[44:47], v0, s[8:11], s42 offen offset:1024\n\t" \
    "s_add_u32 s42, s42, 2048\n\t" \
    "buffer_load_dwordx4 a[48:51], v0, s[8:11], s42 offen\n\t" \
    "buffer_load_dwordx4 a[52:55], v0, s[8:11], s42 offen offset:1024\n\t" \
    "s_add_u32 s42, s42, 2048\n\t" \
    "buffer_load_dwordx4 a[56:59], v0, s[8:11], s42 offen\n\t" \
    "buffer_load_dwordx4 a[60:63], v0, s[8:11], s42 offen offset:1024\n\t" \
    "s_add_u32 s42, s42, 2048\n\t" \
    "buffer_load_dwordx4 a[64:67], v0, s[8:11], s42 offen\n\t" \
    "buffer_load_dwordx4 a[68:71], v0, s[8:11], s42 offen offset:1024\n\t" \
    "s_add_u32 s42, s42, 2048\n\t" \
    "buffer_load_dwordx4 a[72:75], v0, s[8:11], s42 offen\n\t" \
    "buffer_load_dwordx4 a[76:79], v0, s[8:11], s42 offen offset:1024\n\t" \
    "s_add_u32 s42, s42, 2048\n\t" \
    "buffer_load_dwordx4 a[80:83], v0, s[8:11], s42 offen\n\t" \
    "buffer_load_dwordx4 a[84:87], v0, s[8:11], s42 offen offset:1024\n\t" \
    "s_add_u32 s42, s42, 2048\n\t" \
    "buffer_load_dwordx4 a[88:91], v0, s[8:11], s42 offen\n\t" \
    "buffer_load_dwordx4 a[92:95], v0, s[8:11], s42 offen offset:1024\n\t" \
    "s_add_u32 s42, s42, 2048\n\t" \
    "buffer_load_dwordx4 a[96:99], v0, s[8:11], s42 offen\n\t" \
    "buffer_load_dwordx4 a[100:103], v0, s[8:11], s42 offen offset:1024\n\t" \
    "s_add_u32 s42, s42, 2048\n\t" \
    "buffer_load_dwordx4 a[104:107], v0, s[8:11], s42 offen\n\t" \
    "buffer_load_dwordx4 a[108:111], v0, s[8:11], s42 offen offset:1024\n\t" \
    "s_add_u32 s42, s42, 2048\n\t" \
    "buffer_load_dwordx4 a[112:115], v0, s[8:11], s42 offen\n\t" \
    "buffer_load_dwordx4 a[116:119], v0, s[8:11], s42 offen offset:1024\n\t" \
    "s_add_u32 s42, s42, 2048\n\t" \
    "buffer_load_dwordx4 a[120:123], v0, s[8:11], s42 offen\n\t" \
    "buffer_load_dwordx4 a[124:127], v0, s[8:11], s42 offen offset:1024\n\t" \
    "s_add_u32 s42, s42, 2048\n\t" \
    "buffer_load_dwordx4 a[128:131], v0, s[8:11], s42 offen\n\t" \
    "buffer_load_dwordx4 a[132:135], v0, s[8:11], s42 offen offset:1024\n\t" \
    "s_add_u32 s42, s42, 2048\n\t" \
    "buffer_load_dwordx4 a[136:139], v0, s[8:11], s42 offen\n\t" \
    "buffer_load_dwordx4 a[140:143], v0, s[8:11], s42 offen offset:1024\n\t" \
    "s_add_u32 s42, s42, 2048\n\t" \
    "buffer_load_dwordx4 a[144:147], v0, s[8:11], s42 offen\n\t" \
    "buffer_load_dwordx4 a[148:151], v0, s[8:11], s42 offen offset:1024\n\t" \
    "s_add_u32 s42, s42, 2048\n\t" \
    "buffer_load_dwordx4 a[152:155], v0, s[8:11], s42 offen\n\t" \
    "buffer_load_dwordx4 a[156:159], v0, s[8:11], s42 offen offset:1024\n\t" \
    "s_add_u32 s42, s42, 2048\n\t" \
    "buffer_load_dwordx4 a[160:163], v0, s[8:11], s42 offen\n\t" \
    "buffer_load_dwordx4 a[164:167], v0, s[8:11], s42 offen offset:1024\n\t" \
    "s_add_u32 s42, s42, 2048\n\t" \
    "buffer_load_dwordx4 a[168:171], v0, s[8:11], s42 offen\n\t" \
    "buffer_load_dwordx4 a[172:175], v0, s[8:11], s42 offen offset:1024\n\t" \
    "s_add_u32 s42, s42, 2048\n\t" \
    "buffer_load_dwordx4 a[176:179], v0, s[8:11], s42 offen\n\t" \
    "buffer_load_dwordx4 a[180:183], v0, s[8:11], s42 offen offset:1024\n\t" \
    "s_add_u32 s42, s42, 2048\n\t" \
    "buffer_load_dwordx4 a[184:187], v0, s[8:11], s42 offen\n\t" \
    "buffer_load_dwordx4 a[188:191], v0, s[8:11], s42 offen offset:1024\n\t" \
    "s_add_u32 s42, s42, 2048\n\t" \
    "buffer_load_dwordx4 a[192:195], v0, s[8:11], s42 offen\n\t" \
    "buffer_load_dwordx4 a[196:199], v0, s[8:11], s42 offen offset:1024\n\t" \
    "s_add_u32 s42, s42, 2048\n\t" \
    "buffer_load_dwordx4 a[200:203], v0, s[8:11], s42 offen\n\t" \
    "buffer_load_dwordx4 a[204:207], v0, s[8:11], s42 offen offset:1024\n\t" \
    "s_add_u32 s42, s42, 2048\n\t" \
    "buffer_load_dwordx4 a[208:211], v0, s[8:11], s42 offen\n\t" \
    "buffer_load_dwordx4 a[212:215], v0, s[8:11], s42 offen offset:1024\n\t" \
    "s_add_u32 s42, s42, 2048\n\t" \
    "s_waitcnt vmcnt(52)\n\t" \
    "buffer_load_dwordx4 a[216:219], v0, s[8:11], s42 offen\n\t" \
    "buffer_load_dwordx4 a[220:223], v0, s[8:11], s42 offen offset:1024\n\t" \
    "s_add_u32 s42, s42, 2048\n\t" \
    "s_waitcnt vmcnt(52)\n\t" \
    "buffer_load_dwordx4 a[224:227], v0, s[8:11], s42 offen\n\t" \
    "buffer_load_dwordx4 a[228:231], v0, s[8:11], s42 offen offset:1024\n\t" \
    "s_add_u32 s42, s42, 2048\n\t" \
    "s_waitcnt vmcnt(52)\n\t" \
    "buffer_load_dwordx4 a[232:235], v0, s[8:11], s42 offen\n\t" \
    "buffer_load_dwordx4 a[236:239], v0, s[8:11], s42 offen offset:1024\n\t" \
    "s_add_u32 s42, s42, 2048\n\t" \
    "s_waitcnt vmcnt(52)\n\t" \
    "buffer_load_dwordx4 a[240:243], v0, s[8:11], s42 offen\n\t" \
    "buffer_load_dwordx4 a[244:247], v0, s[8:11], s42 offen offset:1024\n\t" \
    "s_add_u32 s42, s42, 2048\n\t" \
    "s_waitcnt vmcnt(52)\n\t" \
    "buffer_load_dwordx4 a[248:251], v0, s[8:11], s42 offen\n\t" \
    "buffer_load_dwordx4 a[252:255], v0, s[8:11], s42 offen offset:1024\n\t" \
    "s_add_u32 s42, s42, 2048\n\t" \
    "s_waitcnt vmcnt(52)\n\t" \
    "buffer_load_dwordx4 v[120:123], v0, s[8:11], s42 offen\n\t" \
    "buffer_load_dwordx4 v[124:127], v0, s[8:11], s42 offen offset:1024\n\t" \
    "s_add_u32 s42, s42, 2048\n\t" \
    "s_waitcnt vmcnt(52)\n\t" \
    "buffer_load_dwordx4 v[128:131], v0, s[8:11], s42 offen\n\t" \
    "buffer_load_dwordx4 v[132:135], v0, s[8:11], s42 offen offset:1024\n\t" \
    "s_add_u32 s42, s42, 2048\n\t" \
    "s_waitcnt vmcnt(52)\n\t" \
    "buffer_load_dwordx4 v[136:139], v0, s[8:11], s42 offen\n\t" \
    "buffer_load_dwordx4 v[140:143], v0, s[8:11], s42 offen offset:1024\n\t" \
    "s_add_u32 s42, s42, 2048\n\t" \
    "s_waitcnt vmcnt(52)\n\t" \
    "buffer_load_dwordx4 v[144:147], v0, s[8:11], s42 offen\n\t" \
    "buffer_load_dwordx4 v[148:151], v0, s[8:11], s42 offen offset:1024\n\t" \
    "s_add_u32 s42, s42, 2048\n\t" \
    "s_waitcnt vmcnt(52)\n\t" \
    "buffer_load_dwordx4 v[152:155], v0, s[8:11], s42 offen\n\t" \
    "buffer_load_dwordx4 v[156:159], v0, s[8:11], s42 offen offset:1024\n\t" \
    "s_add_u32 s42, s42, 2048\n\t" \
    "s_waitcnt vmcnt(52)\n\t" \
    "buffer_load_dwordx4 v[160:163], v0, s[8:11], s42 offen\n\t" \
    "buffer_load_dwordx4 v[164:167], v0, s[8:11], s42 offen offset:1024\n\t" \
    "s_add_u32 s42, s42, 2048\n\t" \
    "s_waitcnt vmcnt(52)\n\t" \
    "buffer_load_dwordx4 v[168:171], v0, s[8:11], s42 offen\n\t" \
    "buffer_load_dwordx4 v[172:175], v0, s[8:11], s42 offen offset:1024\n\t" \
    "s_add_u32 s42, s42, 2048\n\t" \
    "s_waitcnt vmcnt(52)\n\t" \
    "buffer_load_dwordx4 v[176:179], v0, s[8:11], s42 offen\n\t" \
    "buffer_load_dwordx4 v[180:183], v0, s[8:11], s42 offen offset:1024\n\t" \
    "s_add_u32 s42, s42, 2048\n\t" \
    "s_waitcnt vmcnt(52)\n\t" \
    "buffer_load_dwordx4 v[184:187], v0, s[8:11], s42 offen\n\t" \
    "buffer_load_dwordx4 v[188:191], v0, s[8:11], s42 offen offset:1024\n\t" \
    "s_add_u32 s42, s42, 2048\n\t" \
    "s_waitcnt vmcnt(52)\n\t" \
    "buffer_load_dwordx4 v[192:195], v0, s[8:11], s42 offen\n\t" \
    "buffer_load_dwordx4 v[196:199], v0, s[8:11], s42 offen offset:1024\n\t" \
    "s_add_u32 s42, s42, 2048\n\t" \
    "s_waitcnt vmcnt(52)\n\t" \
    "buffer_load_dwordx4 v[200:203], v0, s[8:11], s42 offen\n\t" \
    "buffer_load_dwordx4 v[204:207], v0, s[8:11], s42 offen offset:1024\n\t" \
    "s_add_u32 s42, s42, 2048\n\t" \
    "s_waitcnt vmcnt(52)\n\t" \
    "buffer_load_dwordx4 v[208:211], v0, s[8:11], s42 offen\n\t" \
    "buffer_load_dwordx4 v[212:215], v0, s[8:11], s42 offen offset:1024\n\t" \
    "s_add_u32 s42, s42, 2048\n\t" \
    "s_waitcnt vmcnt(52)\n\t" \
    "buffer_load_dwordx4 v[216:219], v0, s[8:11], s42 offen\n\t" \
    "buffer_load_dwordx4 v[220:223], v0, s[8:11], s42 offen offset:1024\n\t" \
    "s_add_u32 s42, s42, 2048\n\t" \
    "s_waitcnt vmcnt(52)\n\t" \
    "buffer_load_dwordx4 v[224:227], v0, s[8:11], s42 offen\n\t" \
    "buffer_load_dwordx4 v[228:231], v0, s[8:11], s42 offen offset:1024\n\t" \
    "s_add_u32 s42, s42, 2048\n\t" \
    "s_waitcnt vmcnt(52)\n\t" \
    "buffer_load_dwordx4 v[232:235], v0, s[8:11], s42 offen\n\t" \
    "buffer_load_dwordx4 v[236:239], v0, s[8:11], s42 offen offset:1024\n\t" \
    "s_add_u32 s42, s42, 2048\n\t" \
    "s_waitcnt vmcnt(52)\n\t" \
    "buffer_load_dwordx4 v[240:243], v0, s[8:11], s42 offen\n\t" \
    "buffer_load_dwordx4 v[244:247], v0, s[8:11], s42 offen offset:1024\n\t" \
    "s_add_u32 s42, s42, 2048\n\t" \
    "s_waitcnt vmcnt(52)\n\t" \
    "buffer_load_dwordx4 v[248:251], v0, s[8:11], s42 offen\n\t" \
    "buffer_load_dwordx4 v[252:255], v0, s[8:11], s42 offen offset:1024\n\t" \
    "s_add_u32 s42, s42, 2048\n\t" \
    "s_waitcnt vmcnt(52)\n\t" \
    "s_waitcnt vmcnt(0)\n\t" \
    "s_mov_b32 s28, 0\n\t" \
    "s_mov_b32 s31, 0\n\t" \
    "s_mov_b32 s30, 0\n\t" \
    "s_mov_b32 s33, s15\n\t" \
    "s_add_u32 s34, s15, 4096\n\t" \
    "LZ3_loop_%=:\n\t" \
    "s_lshr_b32 s42, s30, 5\n\t" \
    "s_lshl_b32 s42, s42, 2\n\t" \
    "s_and_b32 s45, s30, 1\n\t" \
    "s_lshl_b32 s45, s45, 1\n\t" \
    "s_add_u32 s42, s42, s45\n\t" \
    "s_lshr_b32 s42, s12, s42\n\t" \
    "s_and_b32 s42, s42, 3\n\t" \
    "s_lshl_b32 s42, s42, 13\n\t" \
    "s_lshr_b32 s45, s30, 1\n\t" \
    "s_lshl_b32 s45, s45, 15\n\t" \
    "s_add_u32 s35, s42, s45\n\t" \
    "s_add_u32 s30, s30, 3\n\t" \
    "s_add_u32 s44, s35, 5120\n\t" \
    "s_barrier\n\t" \
    "ds_read_b128 v[80:83], v5 offset:10240\n\t" \
    "ds_read_b128 v[84:87], v5 offset:11264\n\t" \
    "ds_read_b128 v[88:91], v5 offset:24576\n\t" \
    "ds_read_b128 v[92:95], v5 offset:25600\n\t" \
    "ds_read_b128 v[96:99], v5 offset:12288\n\t" \
    "ds_read_b128 v[100:103], v5 offset:13312\n\t" \
    "ds_read_b128 v[104:107], v5 offset:26624\n\t" \
    "ds_read_b128 v[108:111], v5 offset:27648\n\t" \
    "v_mov_b64 v[44:45], 0\n\t" \
    "v_mov_b64 v[46:47], 0\n\t" \
    "v_mov_b64 v[48:49], 0\n\t" \
    "v_mov_b64 v[50:51], 0\n\t" \
    "v_mov_b64 v[52:53], 0\n\t" \
    "v_mov_b64 v[54:55], 0\n\t" \
    "v_mov_b64 v[56:57], 0\n\t" \
    "v_mov_b64 v[58:59], 0\n\t" \
    "v_mov_b64 v[60:61], 0\n\t" \
    "v_mov_b64 v[62:63], 0\n\t" \
    "v_mov_b64 v[64:65], 0\n\t" \
    "v_mov_b64 v[66:67], 0\n\t" \
    "v_mov_b64 v[68:69], 0\n\t" \
    "v_mov_b64 v[70:71], 0\n\t" \
    "v_mov_b64 v[72:73], 0\n\t" \
    "v_mov_b64 v[74:75], 0\n\t" \
    "v_mov_b64 v[76:77], 0\n\t" \
    "v_mov_b64 v[78:79], 0\n\t" \
    "s_mov_b32 s42, s55\n\t" \
    "s_max_i32 s42, s42, 0\n\t" \
    "s_min_i32 s42, s42, 384\n\t" \
    "s_lshl_b32 s42, s42, 2\n\t" \
    "s_sub_u32 s42, 1548, s42\n\t" \
    "s_getpc_b64 s[40:41]\n\t" \
    "s_add_u32 s40, s40, s42\n\t" \
    "s_addc_u32 s41, s41, 0\n\t" \
    "s_setpc_b64 s[40:41]\n\t" \
    ".rept 384\n\t" \
    "s_nop 1\n\t" \
    ".endr\n\t" \
    "LZ3_retry_%=:\n\t" \
    "buffer_load_dwordx4 v[36:39], v0, s[8:11], s44 offen sc1\n\t" \
    "buffer_load_dwordx4 v[40:43], v0, s[8:11], s44 offen offset:1024 sc1\n\t" \
    "s_waitcnt vmcnt(1)\n\t" \
    "ds_write_b128 v6, v[36:39] offset:13312\n\t" \
    "v_max3_u32 v10, v36, v37, v38\n\t" \
    "v_max_u32 v10, v10, v39\n\t" \
    "s_waitcnt vmcnt(0)\n\t" \
    "ds_write_b128 v6, v[40:43] offset:14336\n\t" \
    "v_max3_u32 v10, v40, v41, v10\n\t" \
    "v_max3_u32 v10, v42, v43, v10\n\t" \
    "v_cmp_eq_u32 vcc, -1, v10\n\t" \
    "s_nop 1\n\t" \
    "s_cbranch_vccnz LZ3_fail_%=\n\t" \
    "s_waitcnt lgkmcnt(0)\n\t" \
    "s_barrier\n\t" \
    "ds_read_b128 v[16:19], v6 offset:8192\n\t" \
    "ds_read_b128 v[20:23], v6 offset:9216\n\t" \
    "ds_read_b128 v[24:27], v6 offset:10240\n\t" \
    "ds_read_b128 v[28:31], v6 offset:11264\n\t" \
    "ds_read_b128 v[32:35], v6 offset:12288\n\t" \
    "v_smfmac_f32_16x16x64_bf16 v[44:47], v[36:39], a[40:47], v1\n\t" \
    "v_smfmac_f32_16x16x64_bf16 v[48:51], v[36:39], a[96:103], v1\n\t" \
    "v_smfmac_f32_16x16x64_bf16 v[52:55], v[36:39], a[152:159], v1\n\t" \
    "v_smfmac_f32_16x16x64_bf16 v[56:59], v[36:39], a[208:215], v1\n\t" \
    "v_smfmac_f32_16x16x64_bf16 v[60:63], v[36:39], v[128:135], v1\n\t" \
    "v_smfmac_f32_16x16x64_bf16 v[64:67], v[36:39], v[184:191], v1\n\t" \
    "v_smfmac_f32_16x16x64_bf16 v[68:71], v[36:39], v[240:247], v1\n\t" \
    "v_smfmac_f32_16x16x64_bf16 v[72:75], v[36:39], v[80:87], v1\n\t" \
    "v_smfmac_f32_16x16x64_bf16 v[76:79], v[36:39], v[88:95], v1\n\t" \
    "ds_read_b128 v[80:83], v5 offset:0\n\t" \
    "ds_read_b128 v[84:87], v5 offset:1024\n\t" \
    "ds_read_b128 v[88:91], v5 offset:14336\n\t" \
    "ds_read_b128 v[92:95], v5 offset:15360\n\t" \
    "v_smfmac_f32_16x16x64_bf16 v[44:47], v[40:43], a[48:55], v1\n\t" \
    "v_smfmac_f32_16x16x64_bf16 v[48:51], v[40:43], a[104:111], v1\n\t" \
    "v_smfmac_f32_16x16x64_bf16 v[52:55], v[40:43], a[160:167], v1\n\t" \
    "v_smfmac_f32_16x16x64_bf16 v[56:59], v[40:43], a[216:223], v1\n\t" \
    "v_smfmac_f32_16x16x64_bf16 v[60:63], v[40:43], v[136:143], v1\n\t" \
    "v_smfmac_f32_16x16x64_bf16 v[64:67], v[40:43], v[192:199], v1\n\t" \
    "v_smfmac_f32_16x16x64_bf16 v[68:71], v[40:43], v[248:255], v1\n\t" \
    "v_smfmac_f32_16x16x64_bf16 v[72:75], v[40:43], v[96:103], v1\n\t" \
    "v_smfmac_f32_16x16x64_bf16 v[76:79], v[40:43], v[104:111], v1\n\t" \
    "ds_read_b128 v[96:99], v5 offset:2048\n\t" \
    "ds_read_b128 v[100:103], v5 offset:3072\n\t" \
    "ds_read_b128 v[104:107], v5 offset:16384\n\t" \
    "ds_read_b128 v[108:111], v5 offset:17408\n\t" \
    "s_waitcnt lgkmcnt(12)\n\t" \
    "v_smfmac_f32_16x16x64_bf16 v[44:47], v[16:19], a[0:7], v1\n\t" \
    "v_smfmac_f32_16x16x64_bf16 v[48:51], v[16:19], a[56:63], v1\n\t" \
    "v_smfmac_f32_16x16x64_bf16 v[52:55], v[16:19], a[112:119], v1\n\t" \
    "v_smfmac_f32_16x16x64_bf16 v[56:59], v[16:19], a[168:175], v1\n\t" \
    "v_smfmac_f32_16x16x64_bf16 v[60:63], v[16:19], a[224:231], v1\n\t" \
    "v_smfmac_f32_16x16x64_bf16 v[64:67], v[16:19], v[144:151], v1\n\t" \
    "v_smfmac_f32_16x16x64_bf16 v[68:71], v[16:19], v[200:207], v1\n\t" \
    "s_waitcnt lgkmcnt(4)\n\t" \
    "v_smfmac_f32_16x16x64_bf16 v[72:75], v[16:19], v[80:87], v1\n\t" \
    "v_smfmac_f32_16x16x64_bf16 v[76:79], v[16:19], v[88:95], v1\n\t" \
    "ds_read_b128 v[80:83], v5 offset:4096\n\t" \
    "ds_read_b128 v[84:87], v5 offset:5120\n\t" \
    "ds_read_b128 v[88:91], v5 offset:18432\n\t" \
    "ds_read_b128 v[92:95], v5 offset:19456\n\t" \
    "s_waitcnt lgkmcnt(15)\n\t" \
    "v_smfmac_f32_16x16x64_bf16 v[44:47], v[20:23], a[8:15], v1\n\t" \
    "v_smfmac_f32_16x16x64_bf16 v[48:51], v[20:23], a[64:71], v1\n\t" \
    "v_smfmac_f32_16x16x64_bf16 v[52:55], v[20:23], a[120:127], v1\n\t" \
    "v_smfmac_f32_16x16x64_bf16 v[56:59], v[20:23], a[176:183], v1\n\t" \
    "v_smfmac_f32_16x16x64_bf16 v[60:63], v[20:23], a[232:239], v1\n\t" \
    "v_smfmac_f32_16x16x64_bf16 v[64:67], v[20:23], v[152:159], v1\n\t" \
    "v_smfmac_f32_16x16x64_bf16 v[68:71], v[20:23], v[208:215], v1\n\t" \
    "s_waitcnt lgkmcnt(4)\n\t" \
    "v_smfmac_f32_16x16x64_bf16 v[72:75], v[20:23], v[96:103], v1\n\t" \
    "v_smfmac_f32_16x16x64_bf16 v[76:79], v[20:23], v[104:111], v1\n\t" \
    "ds_read_b128 v[96:99], v5 offset:6144\n\t" \
    "ds_read_b128 v[100:103], v5 offset:7168\n\t" \
    "ds_read_b128 v[104:107], v5 offset:20480\n\t" \
    "ds_read_b128 v[108:111], v5 offset:21504\n\t" \
    "s_waitcnt lgkmcnt(15)\n\t" \
    "v_smfmac_f32_16x16x64_bf16 v[44:47], v[24:27], a[16:23], v1\n\t" \
    "v_smfmac_f32_16x16x64_bf16 v[48:51], v[24:27], a[72:79], v1\n\t" \
    "v_smfmac_f32_16x16x64_bf16 v[52:55], v[24:27], a[128:135], v1\n\t" \
    "v_smfmac_f32_16x16x64_bf16 v[56:59], v[24:27], a[184:191], v1\n\t" \
    "v_smfmac_f32_16x16x64_bf16 v[60:63], v[24:27], a[240:247], v1\n\t" \
    "v_smfmac_f32_16x16x64_bf16 v[64:67], v[24:27], v[160:167], v1\n\t" \
    "v_smfmac_f32_16x16x64_bf16 v[68:71], v[24:27], v[216:223], v1\n\t" \
    "s_waitcnt lgkmcnt(4)\n\t" \
    "v_smfmac_f32_16x16x64_bf16 v[72:75], v[24:27], v[80:87], v1\n\t" \
    "v_smfmac_f32_16x16x64_bf16 v[76:79], v[24:27], v[88:95], v1\n\t" \
    "ds_read_b128 v[80:83], v5 offset:8192\n\t" \
    "ds_read_b128 v[84:87], v5 offset:9216\n\t" \
    "ds_read_b128 v[88:91], v5 offset:22528\n\t" \
    "ds_read_b128 v[92:95], v5 offset:23552\n\t" \
    "s_waitcnt lgkmcnt(15)\n\t" \
    "v_smfmac_f32_16x16x64_bf16 v[44:47], v[28:31], a[24:31], v1\n\t" \
    "v_smfmac_f32_16x16x64_bf16 v[48:51], v[28:31], a[80:87], v1\n\t" \
    "v_smfmac_f32_16x16x64_bf16 v[52:55], v[28:31], a[136:143], v1\n\t" \
    "v_smfmac_f32_16x16x64_bf16 v[56:59], v[28:31], a[192:199], v1\n\t" \
    "v_smfmac_f32_16x16x64_bf16 v[60:63], v[28:31], a[248:255], v1\n\t" \
    "v_smfmac_f32_16x16x64_bf16 v[64:67], v[28:31], v[168:175], v1\n\t" \
    "v_smfmac_f32_16x16x64_bf16 v[68:71], v[28:31], v[224:231], v1\n\t" \
    "s_waitcnt lgkmcnt(4)\n\t" \
    "v_smfmac_f32_16x16x64_bf16 v[72:75], v[28:31], v[96:103], v1\n\t" \
    "v_smfmac_f32_16x16x64_bf16 v[76:79], v[28:31], v[104:111], v1\n\t" \
    "s_waitcnt lgkmcnt(15)\n\t" \
    "v_smfmac_f32_16x16x64_bf16 v[44:47], v[32:35], a[32:39], v1\n\t" \
    "v_smfmac_f32_16x16x64_bf16 v[48:51], v[32:35], a[88:95], v1\n\t" \
    "v_smfmac_f32_16x16x64_bf16 v[52:55], v[32:35], a[144:151], v1\n\t" \
    "v_smfmac_f32_16x16x64_bf16 v[56:59], v[32:35], a[200:207], v1\n\t" \
    "v_smfmac_f32_16x16x64_bf16 v[60:63], v[32:35], v[120:127], v1\n\t" \
    "v_smfmac_f32_16x16x64_bf16 v[64:67], v[32:35], v[176:183], v1\n\t" \
    "v_smfmac_f32_16x16x64_bf16 v[68:71], v[32:35], v[232:239], v1\n\t" \
    "s_waitcnt lgkmcnt(0)\n\t" \
    "v_smfmac_f32_16x16x64_bf16 v[72:75], v[32:35], v[80:87], v1\n\t" \
    "v_smfmac_f32_16x16x64_bf16 v[76:79], v[32:35], v[88:95], v1\n\t" \
    "s_nop 7\n\t" \
    "v_add_f32 v44, v44, v45\n\t" \
    "v_add_f32 v46, v46, v47\n\t" \
    "v_add_f32 v48, v48, v49\n\t" \
    "v_add_f32 v50, v50, v51\n\t" \
    "v_add_f32 v52, v52, v53\n\t" \
    "v_add_f32 v54, v54, v55\n\t" \
    "v_add_f32 v56, v56, v57\n\t" \
    "v_add_f32 v58, v58, v59\n\t" \
    "v_add_f32 v60, v60, v61\n\t" \
    "v_add_f32 v62, v62, v63\n\t" \
    "v_add_f32 v64, v64, v65\n\t" \
    "v_add_f32 v66, v66, v67\n\t" \
    "v_add_f32 v68, v68, v69\n\t" \
    "v_add_f32 v70, v70, v71\n\t" \
    "v_add_f32 v72, v72, v73\n\t" \
    "v_add_f32 v74, v74, v75\n\t" \
    "v_add_f32 v76, v76, v77\n\t" \
    "v_add_f32 v78, v78, v79\n\t" \
    "v_cvt_pk_bf16_f32 v44, v44, v46\n\t" \
    "v_cvt_pk_bf16_f32 v48, v48, v50\n\t" \
    "v_cvt_pk_bf16_f32 v52, v52, v54\n\t" \
    "v_cvt_pk_bf16_f32 v56, v56, v58\n\t" \
    "v_cvt_pk_bf16_f32 v60, v60, v62\n\t" \
    "v_cvt_pk_bf16_f32 v64, v64, v66\n\t" \
    "v_cvt_pk_bf16_f32 v68, v68, v70\n\t" \
    "v_cvt_pk_bf16_f32 v72, v72, v74\n\t" \
    "v_cvt_pk_bf16_f32 v76, v76, v78\n\t" \
    "ds_write_b32 v3, v44 offset:6660\n\t" \
    "ds_write_b32 v3, v48 offset:5120\n\t" \
    "ds_write_b32 v3, v52 offset:7168\n\t" \
    "ds_write_b32 v3, v56 offset:5124\n\t" \
    "ds_write_b32 v3, v60 offset:7172\n\t" \
    "ds_write_b32 v3, v64 offset:5632\n\t" \
    "ds_write_b32 v3, v68 offset:7680\n\t" \
    "ds_write_b32 v3, v72 offset:5636\n\t" \
    "ds_write_b32 v3, v76 offset:7684\n\t" \
    "s_waitcnt lgkmcnt(0)\n\t" \
    "s_barrier\n\t" \
    "ds_read_b128 v[16:19], v6 offset:6144\n\t" \
    "ds_read_b128 v[20:23], v6 offset:7168\n\t" \
    "s_waitcnt lgkmcnt(1)\n\t" \
    "buffer_store_dwordx4 v[16:19], v0, s[8:11], s34 offen offset:2048 sc1\n\t" \
    "s_waitcnt lgkmcnt(0)\n\t" \
    "buffer_store_dwordx4 v[20:23], v0, s[8:11], s34 offen offset:3072 sc1\n\t" \
    "s_add_u32 s28, s28, 1\n\t" \
    "s_cmp_eq_u32 s28, 64\n\t" \
    "s_cbranch_scc1 LZ3_end_%=\n\t" \
    "s_add_u32 s33, s33, 114688\n\t" \
    "s_add_u32 s34, s34, 114688\n\t" \
    "s_branch LZ3_loop_%=\n\t" \
    "LZ3_fail_%=:\n\t" \
    "s_add_u32 s31, s31, 1\n\t" \
    "s_cmp_gt_u32 s31, 131072\n\t" \
    "s_cbranch_scc1 LZ3_end_%=\n\t" \
    "s_branch LZ3_retry_%=\n\t" \
    "LZ3_end_%=:\n\t" \
    "s_waitcnt vmcnt(0)\n\t" \
    "s_barrier\n\t" \
    "s_endpgm\n\t"
#define ASM_CHAIN_CLOBBER \
    "v10", "v11", "v16", "v17", "v18", "v19", "v20", "v21", "v22", "v23", "v24", "v25", "v26", "v27", "v28", "v29", "v30", "v31", "v32", "v33", "v34", "v35", "v36", "v37", \
    "v38", "v39", "v40", "v41", "v42", "v43", "v44", "v45", "v46", "v47", "v48", "v49", "v50", "v51", "v52", "v53", "v54", "v55", "v56", "v57", "v58", "v59", "v60", "v61", \
    "v62", "v63", "v64", "v65", "v66", "v67", "v68", "v69", "v70", "v71", "v72", "v73", "v74", "v75", "v76", "v77", "v78", "v79", "v80", "v81", "v82", "v83", "v84", "v85", \
    "v86", "v87", "v88", "v89", "v90", "v91", "v92", "v93", "v94", "v95", "v96", "v97", "v98", "v99", "v100", "v101", "v102", "v103", "v104", "v105", "v106", "v107", "v108", "v109", \
    "v110", "v111", "v112", "v113", "v114", "v115", "v116", "v117", "v118", "v119", "v120", "v121", "v122", "v123", "v124", "v125", "v126", "v127", "v128", "v129", "v130", "v131", "v132", "v133", \
    "v134", "v135", "v136", "v137", "v138", "v139", "v140", "v141", "v142", "v143", "v144", "v145", "v146", "v147", "v148", "v149", "v150", "v151", "v152", "v153", "v154", "v155", "v156", "v157", \
    "v158", "v159", "v160", "v161", "v162", "v163", "v164", "v165", "v166", "v167", "v168", "v169", "v170", "v171", "v172", "v173", "v174", "v175", "v176", "v177", "v178", "v179", "v180", "v181", \
    "v182", "v183", "v184", "v185", "v186", "v187", "v188", "v189", "v190", "v191", "v192", "v193", "v194", "v195", "v196", "v197", "v198", "v199", "v200", "v201", "v202", "v203", "v204", "v205", \
    "v206", "v207", "v208", "v209", "v210", "v211", "v212", "v213", "v214", "v215", "v216", "v217", "v218", "v219", "v220", "v221", "v222", "v223", "v224", "v225", "v226", "v227", "v228", "v229", \
    "v230", "v231", "v232", "v233", "v234", "v235", "v236", "v237", "v238", "v239", "v240", "v241", "v242", "v243", "v244", "v245", "v246", "v247", "v248", "v249", "v250", "v251", "v252", "v253", \
    "v254", "v255", "a0", "a1", "a2", "a3", "a4", "a5", "a6", "a7", "a8", "a9", "a10", "a11", "a12", "a13", "a14", "a15", "a16", "a17", "a18", "a19", "a20", "a21", \
    "a22", "a23", "a24", "a25", "a26", "a27", "a28", "a29", "a30", "a31", "a32", "a33", "a34", "a35", "a36", "a37", "a38", "a39", "a40", "a41", "a42", "a43", "a44", "a45", \
    "a46", "a47", "a48", "a49", "a50", "a51", "a52", "a53", "a54", "a55", "a56", "a57", "a58", "a59", "a60", "a61", "a62", "a63", "a64", "a65", "a66", "a67", "a68", "a69", \
    "a70", "a71", "a72", "a73", "a74", "a75", "a76", "a77", "a78", "a79", "a80", "a81", "a82", "a83", "a84", "a85", "a86", "a87", "a88", "a89", "a90", "a91", "a92", "a93", \
    "a94", "a95", "a96", "a97", "a98", "a99", "a100", "a101", "a102", "a103", "a104", "a105", "a106", "a107", "a108", "a109", "a110", "a111", "a112", "a113", "a114", "a115", "a116", "a117", \
    "a118", "a119", "a120", "a121", "a122", "a123", "a124", "a125", "a126", "a127", "a128", "a129", "a130", "a131", "a132", "a133", "a134", "a135", "a136", "a137", "a138", "a139", "a140", "a141", \
    "a142", "a143", "a144", "a145", "a146", "a147", "a148", "a149", "a150", "a151", "a152", "a153", "a154", "a155", "a156", "a157", "a158", "a159", "a160", "a161", "a162", "a163", "a164", "a165", \
    "a166", "a167", "a168", "a169", "a170", "a171", "a172", "a173", "a174", "a175", "a176", "a177", "a178", "a179", "a180", "a181", "a182", "a183", "a184", "a185", "a186", "a187", "a188", "a189", \
    "a190", "a191", "a192", "a193", "a194", "a195", "a196", "a197", "a198", "a199", "a200", "a201", "a202", "a203", "a204", "a205", "a206", "a207", "a208", "a209", "a210", "a211", "a212", "a213", \
    "a214", "a215", "a216", "a217", "a218", "a219", "a220", "a221", "a222", "a223", "a224", "a225", "a226", "a227", "a228", "a229", "a230", "a231", "a232", "a233", "a234", "a235", "a236", "a237", \
    "a238", "a239", "a240", "a241", "a242", "a243", "a244", "a245", "a246", "a247", "a248", "a249", "a250", "a251", "a252", "a253", "a254", "a255", "s28", "s29", "s30", "s31", "s32", "s33", \
    "s34", "s35", "s36", "s37", "s38", "s39", "s40", "s41", "s42", "s43", "s44", "s45", "s46", "s47", "s48", "s49", "s50", "s51", "s56", "s57", "s58", "s59", "s60", "s61", \
    "s62", "s63", "s64", "s65", "s66", "s67", "s68", "s69", "s70", "s71", "s72", "s73", "s74", "s75", "s76", "s77", "s78", "s79", "s80", "s81", "s82", "s83", "vcc", "scc", \
    "memory"

constexpr unsigned LA_S = 0;
constexpr unsigned LA_X = LA_S + 8192;
DEV void chain_A(rsrc_t R, uchar* lds, int h, int w, unsigned posbits, int rank PRM_ARG) {
    const int lane = lane_id();
    const unsigned ldsb = (unsigned)(uintptr_t)(LDSP uchar*)lds;
    const unsigned wbase = rfl(OFF_WFA + (h * 4 + w) * (49 * 2048));
    unsigned rel[7];
#pragma unroll
    for (int j = 0; j < 7; ++j) rel[j] = rfl(((j + 2 * w) % 7) * 1024);
    const int g = lane >> 4, col = lane & 15;
    bf16x8 CB;
#pragma unroll
    for (int jj = 0; jj < 8; ++jj) CB[jj] = ((col >> 1) == 2 * g + ((jj >> 1) & 1) && (jj >> 2) == (col & 1)) ? (__bf16)1.0f : (__bf16)0.0f;
    const v4u cb = __builtin_bit_cast(v4u, CB);
    const unsigned idx = (lane & 1) ? 0xEEEEu : 0x4444u;
    unsigned mk[4];
#pragma unroll
    for (int q = 0; q < 4; ++q) {
        const int cs = 4 * q + g, n = col & 7;
        int j, lb, hf = 0;
        if (cs < 8) { j = cs >> 1; lb = cs & 1; }
        else if (cs & 1) { j = 6; lb = (cs - 9) >> 2; hf = ((cs - 9) >> 1) & 1; }
        else { j = 4 + ((cs - 8) >> 2); lb = ((cs - 8) >> 1) & 1; }
        mk[q] = ldsb + LA_X + ((j + 2 * w) % 7) * 1024 + (16 * w + 2 * n + lb) * 16 + hf * 8;
    }
    const unsigned sw = ldsb + LA_S + w * 2048 + 2 * g * 256 + col * 16;
    const int irow = 2 * w + (g & 1);
    const unsigned srd = ldsb + LA_S + irow * 256 + col * 16;
    unsigned pfrag, pin;
    if (lane < 32) { pfrag = 2 + (h >> 1); pin = (16 * (2 * (h & 1) + (col >> 3)) + 2 * irow + ((col >> 2) & 1)) * 16 + (col & 3) * 4; }
    else { pfrag = h >> 3; pin = (16 * ((h >> 1) & 3) + 2 * irow + (h & 1)) * 16 + ((col >> 1) & 3) * 4; }
    const unsigned long long phi = __ballot(pfrag >= 5);
    const unsigned poff = (pfrag % 5) * 1024 + pin;
    const unsigned bias = (col < 8) ? 0u : 0xFF800000u;
    const unsigned xkb = rfl(OFF_XF + w * 8192);
    const unsigned tsb = rfl(OFF_TS + (rank * 4 + w) * 2048);
    const unsigned xl = ldsb + LA_X;
    const unsigned sl0 = rfl(xl + rel[0]), sl1 = rfl(xl + (w == 3 ? 7u * 1024u : rel[1]));
    asm volatile(ASM_CHAIN_A
                 :
                 : "{v0}"(lane * 16), "{v1}"(idx), "{v2}"(mk[0]), "{v3}"(mk[1]), "{v4}"(mk[2]), "{v5}"(mk[3]), "{v6}"(sw), "{v7}"(srd), "{v8}"(poff),
                   "{v9}"(bias), "{v[12:15]}"(cb), "{s[8:11]}"(R), "{s12}"(posbits), "{s13}"(wbase), "{s14}"(xkb), "{s15}"(sl0), "{s16}"(rel[0]),
                   "{s17}"(rel[1]), "{s18}"(rfl(xl + rel[2])), "{s19}"(rfl(xl + rel[3])), "{s20}"(rfl(xl + rel[4])), "{s21}"(rfl(xl + rel[5])),
                   "{s22}"(rfl(xl + rel[6])), "{s24}"(rfl(5120 + h * 128 + w * 4)), "{s[26:27]}"(phi), "{s52}"(P_A0 + P_B0), "{s54}"(tsb),
                   "{s55}"(sl1)
                 : ASM_CHAIN_CLOBBER);
    __builtin_unreachable();
}

constexpr unsigned LB_W = 0;
constexpr unsigned LB_Z = 86016;
constexpr unsigned LB_END = LB_Z + 8192;
DEV void chain_B(rsrc_t R, uchar* lds, const __bf16* mem0, int b, int wv, unsigned posbits, int rank PRM_ARG) {
    const int lane = lane_id();
    const unsigned ldsb = (unsigned)(uintptr_t)(LDSP uchar*)lds;
    const int tl0 = wv ? 5 + 9 * (wv - 1) : 0;
    const unsigned wbase = rfl(OFF_WFB + (b * 32 + tl0) * 14336);
    const int g = lane >> 4, c = lane & 15;
    const unsigned idx = (lane & 1) ? 0xEEEEu : 0x4444u;
    const unsigned moff = (b >> 1) * 1024 + (b & 1) * 512 + (c >> 3) * 256 + (2 * g) * 32 + ((c >> 2) & 1) * 16 + (c & 3) * 4;
    const unsigned zl = ldsb + LB_Z + (g >> 1) * 256 + c * 16 + (g & 1) * 8;
    const unsigned zg = 7168 + (b + lane) * 4;
    const unsigned long long smask = (b == 13) ? 7ull : 1ull;
    const unsigned yb = rfl(OFF_YX + b * YB), zb = rfl(OFF_ZH + b * 8192);
    const unsigned tsb = rfl(OFF_TS + (rank * 4 + wv) * 2048);
    const unsigned yfl = rfl(CTL_YP + (b >> 1) * 128), zfl = rfl(CTL_ZP + b * 4);
#define B_ARGS(V5, V6)                                                                                                                      \
    "{v0}"(lane * 16), "{v1}"(idx), "{v2}"(moff), "{v3}"(zl), "{v4}"(zg), "{v5}"(V5), "{v6}"(V6), "{s[8:11]}"(R), "{s12}"(posbits), \
        "{s13}"(wbase), "{s14}"(yb), "{s15}"(zb), "{s16}"(yfl), "{s17}"(zfl), "{s18}"(P_UP), "{s19}"(P_A0 + P_B0 + P_DN), "{s20}"(14u * YB), "{s23}"(P_B0), "{s[26:27]}"(smask), "{s54}"(tsb), "{s55}"((wv == 0 ? P_DN : P_Z0) / 8)
    if (wv == 0) {
        const unsigned short* m0 = (const unsigned short*)mem0 + (2 * g) * 448 + 32 * b + c;
        const unsigned d0 = min((unsigned)m0[0] | ((unsigned)m0[16] << 16), 0xFFFFFFFEu), d1 = min((unsigned)m0[448] | ((unsigned)m0[464] << 16), 0xFFFFFFFEu);
        st4_sc1(R, moff, ex_chunk(posbits, 0), d0);
        st4_sc1(R, moff + 32, ex_chunk(posbits, 0), d1);
        asm volatile(ASM_CHAIN_PV : : B_ARGS(ldsb + LB_Z + lane * 16, (lane & 15) * 4) : ASM_CHAIN_CLOBBER);
    } else {
        const unsigned wl = LB_W + (wv - 1) * 28672;
#pragma unroll
        for (int q = 0; q < 28; ++q) dma16(R, wbase + 7 * 14336 + q * 1024 + lane * 16, ldsb + wl + q * 1024);
        wait_vm0();
        const unsigned v5 = ldsb + wl + lane * 16;
        if (wv == 1) asm volatile(ASM_CHAIN_Z1 : : B_ARGS(v5, ldsb + LB_Z + lane * 16) : ASM_CHAIN_CLOBBER);
        else if (wv == 2) asm volatile(ASM_CHAIN_Z2 : : B_ARGS(v5, ldsb + LB_Z + lane * 16) : ASM_CHAIN_CLOBBER);
        else asm volatile(ASM_CHAIN_Z3 : : B_ARGS(v5, ldsb + LB_Z + lane * 16) : ASM_CHAIN_CLOBBER);
    }
    __builtin_unreachable();
}

#define XB 1
#define XP_MODE 0
#define POLL_SLEEP 12
DEV bool wait_ge(rsrc_t R, unsigned off, bool on, unsigned need, int slp) {
    for (unsigned spin = 0; spin < SPIN_LIMIT; ++spin) {
        const unsigned v = ld4_poll(R, off);
        if (__all(!on || v >= need)) return true;
        if (slp == 0) __builtin_amdgcn_s_sleep(POLL_SLEEP);
        else __builtin_amdgcn_s_sleep(32);
    }
    return false;
}
DEV void wait_mem(rsrc_t R, int lane, int s) {
    const unsigned off = CTL_ZP + min(lane, 13) * 4;
    const int need = max(s, 1);
    for (unsigned spin = 0; spin < SPIN_LIMIT; ++spin) {
        unsigned v = ld4_poll(R, off);
#pragma unroll
        for (int o = 8; o; o >>= 1) v = max(v, (unsigned)__shfl_xor((int)v, o));
        const int d = need - (int)rfl(v);
        if (d <= 0) return;
        if (d > 1) { for (int q = 1; q < d && q < 6; ++q) __builtin_amdgcn_s_sleep(56); }
        else __builtin_amdgcn_s_sleep(POLL_SLEEP);
    }
}
#define PFS 52
DEV void wait_pf(rsrc_t R, int lane, int s) {
    const int g = (lane >> 2) & 3, q = s - (g == 3 ? 4 : g);
    const unsigned off = CTL_PF + max(q, 0) * 128 + (lane & 3) * 4;
    for (unsigned spin = 0; spin < SPIN_LIMIT; ++spin) {
        const unsigned v = ld4_poll(R, off);
        const unsigned m = (unsigned)__ballot(v >= 1u || q < 0);
        if ((m & 0xFu) == 0xFu) return;
        if ((m & 0xF0u) == 0xF0u) __builtin_amdgcn_s_sleep(POLL_SLEEP);
        else {
            const int n = (m & 0xF00u) == 0xF00u ? 1 : (m & 0xF000u) == 0xF000u ? 2 : 4;
            for (int k = 0; k < n; ++k) __builtin_amdgcn_s_sleep(PFS);
        }
    }
}
#define ACQ 0
#define LDB ld16c
DEV void xs_task(rsrc_t R, int wave, int s, int hq, unsigned posbits) {
    const int lane = lane_id();
    const int h = 4 * hq + wave, r = lane & 31, hh = lane >> 5;
    f32x16 ax, am;
#pragma unroll
    for (int i = 0; i < 16; ++i) { ax[i] = 0.f; am[i] = 0.f; }
    const unsigned kx = OFF_XC + s * 28672 + hh * 512 + perm5(r) * 16;
    const unsigned km = ex_chunk(posbits, 3 * s) + hh * 16 + (r & 7) * 32;
    const unsigned tq = OFF_T + (s * 16 + h) * 28672 + lane * 16;
    bf16x8 bq[28];
    {
        bf16x8 kf[28];
#pragma unroll
        for (int kk = 0; kk < 28; ++kk) { bq[kk] = asb8(LDB(R, tq + kk * 1024)); kf[kk] = asb8(ld16(R, kx + kk * 1024)); }
#pragma unroll
        for (int kk = 0; kk < 28; ++kk) ax = __builtin_amdgcn_mfma_f32_32x32x16_bf16(kf[kk], bq[kk], ax, 0, 0, 0);
    }
    if (wave == 0) wait_mem(R, lane, s);
    raw_barrier();
    {
        bf16x8 kf[28];
#pragma unroll
        for (int kk = 0; kk < 28; ++kk) kf[kk] = asb8(ld16c(R, km + kk * 256));
#pragma unroll
        for (int kk = 0; kk < 28; ++kk) am = __builtin_amdgcn_mfma_f32_32x32x16_bf16(kf[kk], bq[kk], am, 0, 0, 0);
    }
    float mx = fmaxf(fmaxf(am[0], am[1]), fmaxf(am[2], am[3]));
#pragma unroll
    for (int i = 0; i < 16; ++i) {
        const int e = i & 7, key = 16 * (e & 1) + 4 * ((e >> 1) & 1) + (e >> 2) + 8 * (i >> 3) + 2 * hh;
        if (key > r) ax[i] = -INFINITY;
        mx = fmaxf(mx, ax[i]);
    }
    mx = fmaxf(mx, __shfl_xor(mx, 32));
    float sum = 0.f;
#pragma unroll
    for (int i = 0; i < 16; ++i) { ax[i] = __builtin_amdgcn_exp2f(ax[i] - mx); sum += ax[i]; }
#pragma unroll
    for (int i = 0; i < 4; ++i) { am[i] = __builtin_amdgcn_exp2f(am[i] - mx); sum += am[i]; }
    sum += __shfl_xor(sum, 32);
    const float inv = 1.0f / sum;
    const unsigned pb = OFF_PX + (s * 16 + h) * 2560 + r * 16;
    {
        const unsigned pz = OFF_PX + (s * 16 + (h & ~1) + hh) * 2560 + r * 16 + (h & 1) * 4;
        st4_sc1(R, pz, 0, pk(am[0] * inv, am[1] * inv));
        st4_sc1(R, pz + 8, 0, pk(am[2] * inv, am[3] * inv));
    }
#pragma unroll
    for (int x = 0; x < 2; ++x)
        st16_sc1(R, pb + (1 + 2 * x + hh) * 512,
                 (v4u){pk(ax[8 * x] * inv, ax[8 * x + 1] * inv), pk(ax[8 * x + 2] * inv, ax[8 * x + 3] * inv),
                       pk(ax[8 * x + 4] * inv, ax[8 * x + 5] * inv), pk(ax[8 * x + 6] * inv, ax[8 * x + 7] * inv)});
    wait_vm0();
    raw_barrier();
    if (wave == 0 && lane == 0) st4_sc1(R, CTL_PF + s * 128 + hq * 4, 0, 1u);
}

DEV void xo_task(rsrc_t R, uchar* lds, int wave, int s, int b, const __bf16* __restrict__ x, __bf16* __restrict__ out) {
    float (*red)[16][64] = (float (*)[16][64])lds;
    const int lane = lane_id(), r = lane & 31, hh = lane >> 5;
    const int n5 = swap23(r), ct = n5 >> 4, c16 = n5 & 15;
    f32x16 acc;
#pragma unroll
    for (int i = 0; i < 16; ++i) acc[i] = 0.f;
    bf16x8 ya[8];
#pragma unroll
    for (int q = 0; q < 8; ++q) {
        const int h = 4 * wave + (q >> 1), kk = q & 1;
        ya[q] = asb8(LDB(R, OFF_YX + (s * 14 + b) * YB + ((ct * 16 + h) * 4 + 2 * kk + hh) * 256 + c16 * 16));
    }
#pragma unroll
    for (int k0 = 0; k0 < 28; k0 += 14) {
        bf16x8 wa[14], hp[14];
#pragma unroll
        for (int k = 0; k < 14; ++k) {
            const int kk = 28 * wave + k0 + k;
            wa[k] = asb8(ld16(R, OFF_W2F + (b * 112 + kk) * 1024 + lane * 16));
            hp[k] = asb8(LDB(R, OFF_HID + (s * 224 + 2 * kk) * 512 + lane * 16));
        }
#pragma unroll
        for (int k = 0; k < 14; ++k) acc = __builtin_amdgcn_mfma_f32_32x32x16_bf16(wa[k], hp[k], acc, 0, 0, 0);
    }
    const int o = (32 * s + r) * 448 + 32 * b + 16 * (wave & 1) + 8 * hh;
    const v4u xv = *(const v4u*)(x + o);
    if (wave == 0) {
        wait_pf(R, lane, s);
        wait_ge(R, CTL_ZP + b * 4, true, s + 1, 0);
    }
    raw_barrier();
    bf16x8 pa[8], za[2], pz[2];
#pragma unroll
    for (int q = 0; q < 8; ++q) {
        const int h = 4 * wave + (q >> 1), kk = q & 1;
        pa[q] = asb8(ld16c(R, OFF_PX + (s * 16 + h) * 2560 + (1 + 2 * kk + hh) * 512 + r * 16));
    }
#pragma unroll
    for (int mm = 0; mm < 2; ++mm) {
        const int h = 4 * wave + 2 * mm + hh;
        const int p = 2 * wave + mm;
        za[mm] = asb8(ld16c(R, OFF_ZH + (s * 14 + b) * 8192 + ((((p >> 2) * 2 + ct) * 2 + ((p >> 1) & 1)) * 4 + 2 * (p & 1) + hh) * 256 + c16 * 16));
        pz[mm] = asb8(ld16c(R, OFF_PX + (s * 16 + h) * 2560 + r * 16));
    }
#pragma unroll
    for (int q = 0; q < 8; ++q) acc = __builtin_amdgcn_mfma_f32_32x32x16_bf16(ya[q], pa[q], acc, 0, 0, 0);
#pragma unroll
    for (int mm = 0; mm < 2; ++mm) acc = __builtin_amdgcn_mfma_f32_32x32x16_bf16(za[mm], pz[mm], acc, 0, 0, 0);
#pragma unroll
    for (int i = 0; i < 16; ++i) red[wave][i][lane] = acc[i];
    raw_barrier();
    if (wave < 2) {
        float f[8] = {bflo(xv.x), bfhi(xv.x), bflo(xv.y), bfhi(xv.y), bflo(xv.z), bfhi(xv.z), bflo(xv.w), bfhi(xv.w)};
#pragma unroll
        for (int i = 0; i < 8; ++i) f[i] += (red[0][8 * wave + i][lane] + red[1][8 * wave + i][lane]) + (red[2][8 * wave + i][lane] + red[3][8 * wave + i][lane]);
        *(v4u*)(out + o) = (v4u){pk(f[0], f[1]), pk(f[2], f[3]), pk(f[4], f[5]), pk(f[6], f[7])};
    }
    raw_barrier();
}

constexpr int NWORK = 224;
__global__ void __launch_bounds__(256, 1) k_main(const __bf16* __restrict__ x, const __bf16* __restrict__ mem0, uchar* __restrict__ ws,
                                                 __bf16* __restrict__ out PRM_ARG) {
    extern __shared__ __attribute__((aligned(16))) uchar lds[];
    const int rank = blockIdx.x >> 3, wave = rfl(threadIdx.x >> 6);
    const rsrc_t R = mk_rsrc(ws, WS_USED);
    const unsigned posbits = ld_posbits(ws);
    RT_STAMP(0);
    const int xq = blockIdx.x & 7;
    const bool isA = xq == 0 && rank < 16, isB = XB ? (xq == XB && rank < 14) : (xq == 0 && rank >= 16 && rank < 30);
    if (!isA && !isB) {
        const int wi = (xq == 0) ? 192 + rank - 16 : (xq == XB) ? 208 + rank - 14 : rank * 6 + (xq > XB ? xq - 2 : xq - 1);
        if (wi >= NWORK) return;
        if (wi >= 192) return;
        {
            const int c7 = wi / 7, k7 = wi % 7;
            int o, k;
            if (wi < 168 && k7 >= 4 && c7 * 3 + k7 - 4 < 64) { o = (c7 * 3 + k7 - 4) >> 1; k = (k7 - 4 + c7) & 1; }
            else {
                const int j = wi >= 168 ? wi - 72 : k7 >= 4 ? c7 * 3 + k7 - 4 + 56 : c7 * 4 + k7;
                o = j >> 2; k = 2 + (j & 3);
            }
            if (wi < 112) bulk_task<1>(R, lds, wave, wi / 7, wi % 7, 0, 32);
            for (int pass = wi < 112 ? 1 : 0; pass < 2; ++pass) {
                const int idx = pass ? 80 + o : wi - 112;
                const int i0 = pass ? (k < 2 ? 6 * k : 5 * k + 2) : 0, i1 = pass ? (k < 2 ? 6 * k + 6 : 5 * k + 7) : 32;
                bulk_task<0>(R, lds, wave, idx / 7, idx % 7, i0, i1);
            }
            RT_STAMP(1);
            int cb, i0, i1;
            if (wi < 168) { cb = c7; i0 = k7 < 4 ? 5 * k7 : 4 * k7 + 4; i1 = k7 < 4 ? 5 * k7 + 5 : 4 * k7 + 8; }
            else { const int k6 = (wi - 168) % 6; cb = 24 + (wi - 168) / 6; i0 = k6 < 2 ? 6 * k6 : 5 * k6 + 2; i1 = k6 < 2 ? 6 * k6 + 6 : 5 * k6 + 7; }
            bulk_task<2>(R, lds, wave, 0, cb, i0, i1);
        }
        RT_STAMP(2);
        {
            const int lane = lane_id();
            if (wave == 0) {
                if (lane == 0) st4_sc1(R, CTL_BD + wi * 4, 0, 1u);
                for (int q = 0; q < 4; ++q) wait_ge(R, CTL_BD + (q * 64 + lane) * 4, q * 64 + lane < (FREE01 ? 192 : NWORK), 1u, 1);
            }
            raw_barrier();
        }
        RT_STAMP(3);
        if (wi >= NW2) return;
        const int wj = wi;
        if (wj < NXS) {
            for (int t = wj; t < NSEG * 4; t += NXS) { xs_task(R, wave, t >> 2, t & 3, posbits); RT_STAMP(min(4 + t / NXS, 9)); }
        } else {
            for (int t = wj - NXS; t < NSEG * 14; t += NW2 - NXS) { xo_task(R, lds, wave, t / 14, t % 14, x, out); RT_STAMP(min(4 + t / (NW2 - NXS), 9)); }
        }
        return;
        for (int t = wj; t < NSEG * 18; t += NW2) {
            const int s = t / 18, j = t % 18;
            if (j < 4) xs_task(R, wave, s, j, posbits);
            else xo_task(R, lds, wave, s, j - 4, x, out);
            RT_STAMP(min(4 + t / NW2, 9));
        }
        return;
    }
    if (isA) chain_A(R, lds, rank, wave, posbits, rank PRM_PASS);
    else { const int b = XB ? rank : rank - 16; chain_B(R, lds, mem0, b, wave, posbits, 16 + b PRM_PASS); }
}

extern "C" void kernel_launch(void* const* d_in, const int* in_sizes, int n_in, void* d_out, int out_size, void* d_ws, size_t ws_size, hipStream_t stream) {
    const __bf16* x = (const __bf16*)d_in[0];
    const __bf16* mem0 = (const __bf16*)d_in[1];
    const __bf16* wq = (const __bf16*)d_in[2];
    const __bf16* wk = (const __bf16*)d_in[3];
    const __bf16* wv = (const __bf16*)d_in[4];
    const __bf16* wo = (const __bf16*)d_in[5];
    const __bf16* w1 = (const __bf16*)d_in[8];
    const __bf16* w2 = (const __bf16*)d_in[9];
    uchar* ws = (uchar*)(((uintptr_t)d_ws + 524287u) & ~(uintptr_t)524287u);
    (void)hipFuncSetAttribute((const void*)k_main, hipFuncAttributeMaxDynamicSharedMemorySize, 114688);
    (void)hipFuncSetAttribute((const void*)k_prep, hipFuncAttributeMaxDynamicSharedMemorySize, PREP_LDS);
    k_prep<<<256, PREP_THREADS, PREP_LDS, stream>>>(x, wq, wk, wv, wo, w1, w2, ws);
    k_main<<<256, 256, 114688, stream>>>(x, mem0, ws, (__bf16*)d_out PRM_HOST);
}
```
